```python
import torch

def _softmax(x, axis=-1):
    return torch.softmax(x, dim=axis)
def _gelu(x):
    return torch.nn.functional.gelu(x, approximate="tanh")
def _silu(x):
    return torch.nn.functional.silu(x)
def _sigmoid(x):
    return torch.sigmoid(x)
def _layernorm(x, axis=-1, eps=1e-5):
    m = x.mean(dim=axis, keepdim=True)
    v = x.var(dim=axis, keepdim=True, unbiased=False)
    return (x - m) / torch.sqrt(v + eps)
def _scatter_add(tgt, idx, src, axis=0):
    return tgt.clone().index_add_(axis, idx, src)
def _segment_sum(src, seg, n):
    out = torch.zeros((n,) + src.shape[1:], dtype=src.dtype)
    return out.index_add_(0, seg, src)
def _pad(x, pw):
    flat = tuple(v for pair in reversed(pw) for v in pair)
    return torch.nn.functional.pad(x, flat)

kernel_name = 'standalone__qk_norm__prompt'

def setup_inputs():

    _w = (lambda a: (lambda t: t.to(torch.bfloat16) if t.is_floating_point() else t)(torch.as_tensor(a)))
    B, H, S, D = 4, 16, 2048, 448
    Dh = D // H
    return {
        "q": _w((lambda *s: torch.randn(*s, dtype=torch.bfloat16) * 0.05)(B, H, S, Dh)),
        "k": _w((lambda *s: torch.randn(*s, dtype=torch.bfloat16) * 0.05)(B, H, S, Dh)),
        "v": _w((lambda *s: torch.randn(*s, dtype=torch.bfloat16) * 0.8)(B, H, S, Dh)),
        "t": _w(torch.abs((lambda *s: torch.randn(*s, dtype=torch.bfloat16) * 0.8)(H)) + 1.0),
    }

def reference(q, k, v, t):
    eps = 1e-6
    qn = q / (torch.sqrt((q * q).sum(axis=-1, keepdims=True)) + eps)
    kn = k / (torch.sqrt((k * k).sum(axis=-1, keepdims=True)) + eps)
    logits = torch.einsum('bhqd,bhkd->bhqk', qn, kn) * t[None, :, None, None]
    attn = _softmax(logits, axis=-1)
    return torch.einsum('bhqk,bhkd->bhqd', attn, v)
```

```cpp
#include <hip/hip_runtime.h>
#include <hip/hip_bf16.h>

typedef _Float16 f16x8 __attribute__((ext_vector_type(8)));
typedef _Float16 f16x2 __attribute__((ext_vector_type(2)));
typedef __bf16 bf16x8 __attribute__((ext_vector_type(8)));
typedef __bf16 bf16x2 __attribute__((ext_vector_type(2)));
typedef short s16x4 __attribute__((ext_vector_type(4)));
typedef float f32x16 __attribute__((ext_vector_type(16)));
typedef float f32x2 __attribute__((ext_vector_type(2)));
typedef unsigned u32x4 __attribute__((ext_vector_type(4)));
typedef unsigned u32x2 __attribute__((ext_vector_type(2)));
typedef unsigned u32x4a8 __attribute__((ext_vector_type(4), aligned(8)));

#define DI __device__ __forceinline__
#define LDSP(T) __attribute__((address_space(3))) T*

constexpr int SEQ = 2048, DH = 28, NHEAD = 16, ROWB = DH * 2;
constexpr int CHUNK = 512, NCHUNK = SEQ / CHUNK, TPC = CHUNK / 32;
constexpr int STAGE = 65536, VOFF = 32768, LDS_BYTES = 2 * STAGE;
constexpr float LOG2E = 1.4426950408889634f;
constexpr float FAST_LIMIT = 24.0f;
constexpr float EPS = 1e-6f;

DI unsigned pk_bf16(float lo, float hi) { f32x2 v = {lo, hi}; return __builtin_bit_cast(unsigned, __builtin_convertvector(v, bf16x2)); }
DI unsigned pk_f16(float lo, float hi) { f32x2 v = {lo, hi}; return __builtin_bit_cast(unsigned, __builtin_convertvector(v, f16x2)); }
DI float bf_lo(unsigned w) { return __uint_as_float(w << 16); }
DI float bf_hi(unsigned w) { return __uint_as_float(w & 0xffff0000u); }
DI float dot2(unsigned a, float c) {
  return __builtin_amdgcn_fdot2_f32_bf16(__builtin_bit_cast(bf16x2, a), __builtin_bit_cast(bf16x2, a), c, false);
}
DI unsigned scale_pk(unsigned w, float r) { return pk_f16(bf_lo(w) * r, bf_hi(w) * r); }
DI float swap_sum(float x) {
  auto rr = __builtin_amdgcn_permlane32_swap(__float_as_uint(x), __float_as_uint(x), false, false);
  return __uint_as_float(rr[0]) + __uint_as_float(rr[1]);
}
DI float swap_max(float x) {
  auto rr = __builtin_amdgcn_permlane32_swap(__float_as_uint(x), __float_as_uint(x), false, false);
  return fmaxf(__uint_as_float(rr[0]), __uint_as_float(rr[1]));
}
DI f32x16 mfma_qk(f16x8 a, f16x8 b, f32x16 c) { return __builtin_amdgcn_mfma_f32_32x32x16_f16(a, b, c, 0, 0, 0); }
DI f32x16 mfma_pv(bf16x8 a, bf16x8 b, f32x16 c) { return __builtin_amdgcn_mfma_f32_32x32x16_bf16(a, b, c, 0, 0, 0); }

struct KRaw { u32x4 a, b, c; u32x2 d; };

DI KRaw k_load(const char* p) {
  KRaw r;
  r.a = *(const u32x4a8*)(p); r.b = *(const u32x4a8*)(p + 16); r.c = *(const u32x4a8*)(p + 32); r.d = *(const u32x2*)(p + 48);
  return r;
}
DI void k_norm_store(const KRaw& k, LDSP(char) dst, unsigned pad0) {
  float s0 = dot2(k.a[0], 0.f), s1 = dot2(k.a[1], 0.f);
  s0 = dot2(k.a[2], s0); s1 = dot2(k.a[3], s1);
  s0 = dot2(k.b[0], s0); s1 = dot2(k.b[1], s1); s0 = dot2(k.b[2], s0); s1 = dot2(k.b[3], s1);
  s0 = dot2(k.c[0], s0); s1 = dot2(k.c[1], s1); s0 = dot2(k.c[2], s0); s1 = dot2(k.c[3], s1);
  s0 = dot2(k.d[0], s0); s1 = dot2(k.d[1], s1);
  const float r = __builtin_amdgcn_rcpf(__builtin_amdgcn_sqrtf(s0 + s1) + EPS);
  u32x4 o;
  o = (u32x4){scale_pk(k.a[0], r), scale_pk(k.a[1], r), scale_pk(k.a[2], r), scale_pk(k.a[3], r)};
  *(LDSP(u32x4))(dst) = o;
  o = (u32x4){scale_pk(k.b[0], r), scale_pk(k.b[1], r), scale_pk(k.b[2], r), scale_pk(k.b[3], r)};
  *(LDSP(u32x4))(dst + CHUNK * 16) = o;
  o = (u32x4){scale_pk(k.c[0], r), scale_pk(k.c[1], r), scale_pk(k.c[2], r), scale_pk(k.c[3], r)};
  *(LDSP(u32x4))(dst + CHUNK * 32) = o;
  o = (u32x4){scale_pk(k.d[0], r), scale_pk(k.d[1], r), pad0, 0u};
  *(LDSP(u32x4))(dst + CHUNK * 48) = o;
}

DI void v_dma(const char* gbase  , unsigned voff  , unsigned ldsdst  ) {
  unsigned keep; unsigned long long ex;
  asm volatile(
      "s_mov_b32 %0, m0\n\t"
      "s_mov_b64 %1, exec\n\t"
      "s_mov_b32 exec_lo, 0x3fff3fff\n\t"
      "s_mov_b32 exec_hi, 0x3fff3fff\n\t"
      "s_mov_b32 m0, %4\n\t"
      "s_nop 0\n\t"
      "global_load_lds_dword %2, %3 offset:0\n\t"    "s_add_u32 m0, m0, 32\n\t" "s_nop 0\n\t"
      "global_load_lds_dword %2, %3 offset:224\n\t"  "s_add_u32 m0, m0, 32\n\t" "s_nop 0\n\t"
      "global_load_lds_dword %2, %3 offset:448\n\t"  "s_add_u32 m0, m0, 32\n\t" "s_nop 0\n\t"
      "global_load_lds_dword %2, %3 offset:672\n\t"  "s_add_u32 m0, m0, 32\n\t" "s_nop 0\n\t"
      "global_load_lds_dword %2, %3 offset:896\n\t"  "s_add_u32 m0, m0, 32\n\t" "s_nop 0\n\t"
      "global_load_lds_dword %2, %3 offset:1120\n\t" "s_add_u32 m0, m0, 32\n\t" "s_nop 0\n\t"
      "global_load_lds_dword %2, %3 offset:1344\n\t" "s_add_u32 m0, m0, 32\n\t" "s_nop 0\n\t"
      "global_load_lds_dword %2, %3 offset:1568\n\t" "s_add_u32 m0, m0, 32\n\t" "s_nop 0\n\t"
      "global_load_lds_dword %2, %3 offset:1792\n\t" "s_add_u32 m0, m0, 32\n\t" "s_nop 0\n\t"
      "global_load_lds_dword %2, %3 offset:2016\n\t" "s_add_u32 m0, m0, 32\n\t" "s_nop 0\n\t"
      "global_load_lds_dword %2, %3 offset:2240\n\t" "s_add_u32 m0, m0, 32\n\t" "s_nop 0\n\t"
      "global_load_lds_dword %2, %3 offset:2464\n\t" "s_add_u32 m0, m0, 32\n\t" "s_nop 0\n\t"
      "global_load_lds_dword %2, %3 offset:2688\n\t" "s_add_u32 m0, m0, 32\n\t" "s_nop 0\n\t"
      "global_load_lds_dword %2, %3 offset:2912\n\t" "s_add_u32 m0, m0, 32\n\t" "s_nop 0\n\t"
      "global_load_lds_dword %2, %3 offset:3136\n\t" "s_add_u32 m0, m0, 32\n\t" "s_nop 0\n\t"
      "global_load_lds_dword %2, %3 offset:3360\n\t"
      "s_mov_b64 exec, %1\n\t"
      "s_mov_b32 m0, %0"
      : "=&s"(keep), "=&s"(ex)
      : "v"(voff), "s"(gbase), "s"(ldsdst)
      : "memory", "scc");
}

DI s16x4 vtr(LDSP(const char) p) {
  typedef short v4s __attribute__((ext_vector_type(4)));
  return __builtin_amdgcn_ds_read_tr16_b64_v4i16((LDSP(v4s))p);
}
DI bf16x8 vfrag(LDSP(const char) p) {
  s16x4 lo = vtr(p), hi = vtr(p + 512);
  return __builtin_bit_cast(bf16x8, __builtin_shufflevector(lo, hi, 0, 1, 2, 3, 4, 5, 6, 7));
}
DI f16x8 kfrag(LDSP(const char) p) { return *(LDSP(const f16x8))p; }

DI void exp_pack(f32x16& s, bf16x8& p0, bf16x8& p1) {
#pragma unroll
  for (int i = 0; i < 16; ++i) s[i] = __builtin_amdgcn_exp2f(s[i]);
  u32x4 a = {pk_bf16(s[0], s[1]), pk_bf16(s[2], s[3]), pk_bf16(s[4], s[5]), pk_bf16(s[6], s[7])};
  u32x4 b = {pk_bf16(s[8], s[9]), pk_bf16(s[10], s[11]), pk_bf16(s[12], s[13]), pk_bf16(s[14], s[15])};
  p0 = __builtin_bit_cast(bf16x8, a); p1 = __builtin_bit_cast(bf16x8, b);
}

template <bool FAST>
DI void body(const char* __restrict__ Qh, const char* __restrict__ Kh, const char* __restrict__ Vh, char* __restrict__ Oh,
             int q0, float c, LDSP(char) lds) {
  const int tid = threadIdx.x, lane = tid & 63, r = lane & 31, h = lane >> 5;
  const int wid = __builtin_amdgcn_readfirstlane(tid >> 6);
  const unsigned lds0 = (unsigned)(size_t)lds;

  const unsigned voff = (unsigned)((lane >> 4) * ROWB + (lane & 15) * 4);
  v_dma(Vh + (size_t)wid * 64 * ROWB, voff, lds0 + VOFF + wid * 64 * 64);
  const char* krow = Kh + (size_t)tid * ROWB;
  KRaw kr = k_load(krow);
  u32x4 qa[2]; u32x2 qb[2], qc[2];
#pragma unroll
  for (int b = 0; b < 2; ++b) {
    const char* qp = Qh + (size_t)(q0 + wid * 64 + b * 32 + r) * ROWB + h * 16;
    qa[b] = *(const u32x4a8*)qp;
    qb[b] = *(const u32x2*)(qp + 32);
    qc[b] = *(const u32x2*)(qp + (h ? 32 : 40));
  }
  {
    const u32x2 one = {0x00003F80u, 0u};
    *(LDSP(u32x2))(lds + VOFF + tid * 64 + 56) = one;
    *(LDSP(u32x2))(lds + STAGE + VOFF + tid * 64 + 56) = one;
  }
  f16x8 qf[2][2];
  const unsigned hmask = h ? 0u : 0xffffffffu;
  const float qscale = FAST ? c : 1.0f;
  const unsigned qpad = h ? 0x00003C00u : 0u;
#pragma unroll
  for (int b = 0; b < 2; ++b) {
    qc[b][0] &= hmask; qc[b][1] &= hmask;
    float s0 = dot2(qa[b][0], 0.f), s1 = dot2(qa[b][1], 0.f);
    s0 = dot2(qa[b][2], s0); s1 = dot2(qa[b][3], s1);
    s0 = dot2(qb[b][0], s0); s1 = dot2(qb[b][1], s1);
    s0 = dot2(qc[b][0], s0); s1 = dot2(qc[b][1], s1);
    const float ss = swap_sum(s0 + s1);
    const float rq = qscale * __builtin_amdgcn_rcpf(__builtin_amdgcn_sqrtf(ss) + EPS);
    u32x4 x = {scale_pk(qa[b][0], rq), scale_pk(qa[b][1], rq), scale_pk(qa[b][2], rq), scale_pk(qa[b][3], rq)};
    u32x4 y = {scale_pk(qb[b][0], rq), scale_pk(qb[b][1], rq), scale_pk(qc[b][0], rq) | qpad, scale_pk(qc[b][1], rq)};
    qf[b][0] = __builtin_bit_cast(f16x8, x); qf[b][1] = __builtin_bit_cast(f16x8, y);
  }
  const unsigned kpad = FAST ? (pk_f16(-fabsf(c), 0.f)) : 0u;
  k_norm_store(kr, lds + tid * 16, kpad);

  f32x16 o0 = {}, o1 = {};
  float m0 = -INFINITY, m1 = -INFINITY;
  const unsigned kl = h * (CHUNK * 16) + r * 16;
  const unsigned vl = VOFF + (4 * h + ((lane & 15) >> 2)) * 64 + ((lane >> 4) & 1) * 32 + (lane & 3) * 8;

  for (int ch = 0; ch < NCHUNK; ++ch) {
    asm volatile("s_waitcnt vmcnt(0) lgkmcnt(0)\n\ts_barrier" ::: "memory");
    const int st = (ch & 1) * STAGE, nst = STAGE - st;
    const bool more = ch + 1 < NCHUNK;
    if (more) {
      v_dma(Vh + (size_t)((ch + 1) * CHUNK + wid * 64) * ROWB, voff, lds0 + nst + VOFF + wid * 64 * 64);
      kr = k_load(krow + (size_t)(ch + 1) * CHUNK * ROWB);
    }
    LDSP(const char) kp = lds + st + kl;
    LDSP(const char) vp = lds + st + vl;
    if (FAST) {
#pragma unroll 4
      for (int t = 0; t < TPC; ++t) {
        const f16x8 k0 = kfrag(kp + t * 512), k1 = kfrag(kp + t * 512 + CHUNK * 32);
        const bf16x8 v0 = vfrag(vp + t * 2048), v1 = vfrag(vp + t * 2048 + 1024);
        f32x16 sa = mfma_qk(k0, qf[0][0], (f32x16){}); sa = mfma_qk(k1, qf[0][1], sa);
        f32x16 sb = mfma_qk(k0, qf[1][0], (f32x16){}); sb = mfma_qk(k1, qf[1][1], sb);
        bf16x8 p0, p1;
        exp_pack(sa, p0, p1);
        o0 = mfma_pv(v0, p0, o0); o0 = mfma_pv(v1, p1, o0);
        exp_pack(sb, p0, p1);
        o1 = mfma_pv(v0, p0, o1); o1 = mfma_pv(v1, p1, o1);
      }
    } else {
#pragma unroll 1
      for (int t = 0; t < TPC; ++t) {
        const f16x8 k0 = kfrag(kp + t * 512), k1 = kfrag(kp + t * 512 + CHUNK * 32);
        const bf16x8 v0 = vfrag(vp + t * 2048), v1 = vfrag(vp + t * 2048 + 1024);
#pragma unroll
        for (int b = 0; b < 2; ++b) {
          f32x16& o = b ? o1 : o0; float& m = b ? m1 : m0;
          f32x16 s = mfma_qk(k0, qf[b][0], (f32x16){}); s = mfma_qk(k1, qf[b][1], s);
          float mx = m;
#pragma unroll
          for (int i = 0; i < 16; ++i) { s[i] *= c; mx = fmaxf(mx, s[i]); }
          mx = swap_max(mx);
          const float al = __builtin_amdgcn_exp2f(m - mx);
          m = mx;
#pragma unroll
          for (int i = 0; i < 16; ++i) { s[i] -= mx; o[i] *= al; }
          bf16x8 p0, p1;
          exp_pack(s, p0, p1);
          o = mfma_pv(v0, p0, o); o = mfma_pv(v1, p1, o);
        }
      }
    }
    if (more) k_norm_store(kr, lds + nst + tid * 16, kpad);
  }

#pragma unroll
  for (int b = 0; b < 2; ++b) {
    const f32x16& o = b ? o1 : o0;
    auto rr = __builtin_amdgcn_permlane32_swap(__float_as_uint(o[12]), __float_as_uint(o[12]), false, false);
    const float rl = 1.0f / __uint_as_float(rr[1]);
    char* op = Oh + (size_t)(q0 + wid * 64 + b * 32 + r) * ROWB + h * 8;
#pragma unroll
    for (int g = 0; g < 4; ++g) {
      const u32x2 w = {pk_bf16(o[4 * g] * rl, o[4 * g + 1] * rl), pk_bf16(o[4 * g + 2] * rl, o[4 * g + 3] * rl)};
      if (g < 3 || h == 0) *(u32x2*)(op + g * 16) = w;
    }
  }
}

__global__ __launch_bounds__(512, 2) void qkn_attn(const char* __restrict__ q, const char* __restrict__ k, const char* __restrict__ v,
                                                   const unsigned short* __restrict__ t, char* __restrict__ out) {
  extern __shared__ __attribute__((aligned(16))) char smem[];
  const int bid = blockIdx.x, x = bid & 7, y = bid >> 3;
  const int head = x * 8 + (y >> 2), q0 = (y & 3) * 512;
  const size_t hoff = (size_t)head * SEQ * ROWB;
  const float c = __uint_as_float((unsigned)t[head & (NHEAD - 1)] << 16) * LOG2E;
  LDSP(char) lds = (LDSP(char))smem;
  if (fabsf(c) <= FAST_LIMIT) body<true>(q + hoff, k + hoff, v + hoff, out + hoff, q0, c, lds);
  else body<false>(q + hoff, k + hoff, v + hoff, out + hoff, q0, c, lds);
}

extern "C" void kernel_launch(void* const* d_in, const int* in_sizes, int n_in, void* d_out, int out_size, void* d_ws, size_t ws_size,
                              hipStream_t stream) {
  (void)hipFuncSetAttribute((const void*)qkn_attn, hipFuncAttributeMaxDynamicSharedMemorySize, LDS_BYTES);
  qkn_attn<<<256, 512, LDS_BYTES, stream>>>((const char*)d_in[0], (const char*)d_in[1], (const char*)d_in[2],
                                            (const unsigned short*)d_in[3], (char*)d_out);
}
```

```cpp
#include <hip/hip_runtime.h>
#include <hip/hip_bf16.h>

typedef _Float16 f16x8 __attribute__((ext_vector_type(8)));
typedef _Float16 f16x2 __attribute__((ext_vector_type(2)));
typedef __bf16 bf16x8 __attribute__((ext_vector_type(8)));
typedef __bf16 bf16x2 __attribute__((ext_vector_type(2)));
typedef short s16x4 __attribute__((ext_vector_type(4)));
typedef float f32x16 __attribute__((ext_vector_type(16)));
typedef float f32x2 __attribute__((ext_vector_type(2)));
typedef unsigned u32x4 __attribute__((ext_vector_type(4)));
typedef unsigned u32x2 __attribute__((ext_vector_type(2)));
typedef unsigned u32x4a8 __attribute__((ext_vector_type(4), aligned(8)));

#define DI __device__ __forceinline__
#define LDSP(T) __attribute__((address_space(3))) T*

constexpr int SEQ = 2048, DH = 28, NHEAD = 16, ROWB = DH * 2;
constexpr int CHUNK = 256, NCHUNK = SEQ / CHUNK, TPC = CHUNK / 32;
constexpr int BLK = 4096, STAGE = TPC * BLK, NSTAGE = 4, LDS_BYTES = NSTAGE * STAGE;
constexpr float LOG2E = 1.4426950408889634f;
constexpr float FAST_LIMIT = 24.0f;
constexpr float EPS = 1e-6f;

DI unsigned pk_bf16(float lo, float hi) { f32x2 v = {lo, hi}; return __builtin_bit_cast(unsigned, __builtin_convertvector(v, bf16x2)); }
DI unsigned pk_f16(float lo, float hi) { f32x2 v = {lo, hi}; return __builtin_bit_cast(unsigned, __builtin_convertvector(v, f16x2)); }
DI float bf_lo(unsigned w) { return __uint_as_float(w << 16); }
DI float bf_hi(unsigned w) { return __uint_as_float(w & 0xffff0000u); }
DI float dot2(unsigned a, float c) {
  return __builtin_amdgcn_fdot2_f32_bf16(__builtin_bit_cast(bf16x2, a), __builtin_bit_cast(bf16x2, a), c, false);
}
DI unsigned scale_pk(unsigned w, float r) { return pk_bf16(bf_lo(w) * r, bf_hi(w) * r); }
DI float swap_sum(float x) {
  auto rr = __builtin_amdgcn_permlane32_swap(__float_as_uint(x), __float_as_uint(x), false, false);
  return __uint_as_float(rr[0]) + __uint_as_float(rr[1]);
}
DI float swap_max(float x) {
  auto rr = __builtin_amdgcn_permlane32_swap(__float_as_uint(x), __float_as_uint(x), false, false);
  return fmaxf(__uint_as_float(rr[0]), __uint_as_float(rr[1]));
}
DI f32x16 mfma_qk(f16x8 a, f16x8 b, f32x16 c) { return __builtin_amdgcn_mfma_f32_32x32x16_f16(a, b, c, 0, 0, 0); }
DI f32x16 mfma_pv(bf16x8 a, bf16x8 b, f32x16 c) { return __builtin_amdgcn_mfma_f32_32x32x16_bf16(a, b, c, 0, 0, 0); }

struct KRaw { u32x4 a, b, c; u32x2 d; };

DI KRaw k_load(const char* p) {
  KRaw r;
  r.a = *(const u32x4a8*)(p); r.b = *(const u32x4a8*)(p + 16); r.c = *(const u32x4a8*)(p + 32); r.d = *(const u32x2*)(p + 48);
  return r;
}
DI void k_norm_store(const KRaw& k, LDSP(char) dst, unsigned pad0) {
  float s0 = dot2(k.a[0], 0.f), s1 = dot2(k.a[1], 0.f);
  s0 = dot2(k.a[2], s0); s1 = dot2(k.a[3], s1);
  s0 = dot2(k.b[0], s0); s1 = dot2(k.b[1], s1); s0 = dot2(k.b[2], s0); s1 = dot2(k.b[3], s1);
  s0 = dot2(k.c[0], s0); s1 = dot2(k.c[1], s1); s0 = dot2(k.c[2], s0); s1 = dot2(k.c[3], s1);
  s0 = dot2(k.d[0], s0); s1 = dot2(k.d[1], s1);
  const float r = __builtin_amdgcn_rcpf(__builtin_amdgcn_sqrtf(s0 + s1) + EPS);
  u32x4 o;
  o = (u32x4){scale_pk(k.a[0], r), scale_pk(k.a[1], r), scale_pk(k.a[2], r), scale_pk(k.a[3], r)};
  *(LDSP(u32x4))(dst) = o;
  o = (u32x4){scale_pk(k.b[0], r), scale_pk(k.b[1], r), scale_pk(k.b[2], r), scale_pk(k.b[3], r)};
  *(LDSP(u32x4))(dst + 512) = o;
  o = (u32x4){scale_pk(k.c[0], r), scale_pk(k.c[1], r), scale_pk(k.c[2], r), scale_pk(k.c[3], r)};
  *(LDSP(u32x4))(dst + 1024) = o;
  o = (u32x4){scale_pk(k.d[0], r), scale_pk(k.d[1], r), pad0, 0u};
  *(LDSP(u32x4))(dst + 1536) = o;
}

DI void v_dma(const char* gbase  , unsigned voff  , unsigned ldsdst  ) {
  unsigned keep; unsigned long long ex;
  asm volatile(
      "s_mov_b32 %0, m0\n\t"
      "s_mov_b64 %1, exec\n\t"
      "s_mov_b32 exec_lo, 0x3fff3fff\n\t"
      "s_mov_b32 exec_hi, 0x3fff3fff\n\t"
      "s_mov_b32 m0, %4\n\t"
      "s_nop 0\n\t"
      "global_load_lds_dword %2, %3 offset:0\n\t"    "s_add_u32 m0, m0, 32\n\t" "s_nop 0\n\t"
      "global_load_lds_dword %2, %3 offset:224\n\t"  "s_add_u32 m0, m0, 32\n\t" "s_nop 0\n\t"
      "global_load_lds_dword %2, %3 offset:448\n\t"  "s_add_u32 m0, m0, 32\n\t" "s_nop 0\n\t"
      "global_load_lds_dword %2, %3 offset:672\n\t"  "s_add_u32 m0, m0, 32\n\t" "s_nop 0\n\t"
      "global_load_lds_dword %2, %3 offset:896\n\t"  "s_add_u32 m0, m0, 32\n\t" "s_nop 0\n\t"
      "global_load_lds_dword %2, %3 offset:1120\n\t" "s_add_u32 m0, m0, 32\n\t" "s_nop 0\n\t"
      "global_load_lds_dword %2, %3 offset:1344\n\t" "s_add_u32 m0, m0, 32\n\t" "s_nop 0\n\t"
      "global_load_lds_dword %2, %3 offset:1568\n\t" "s_add_u32 m0, m0, 2080\n\t" "s_nop 0\n\t"
      "global_load_lds_dword %2, %3 offset:1792\n\t" "s_add_u32 m0, m0, 32\n\t" "s_nop 0\n\t"
      "global_load_lds_dword %2, %3 offset:2016\n\t" "s_add_u32 m0, m0, 32\n\t" "s_nop 0\n\t"
      "global_load_lds_dword %2, %3 offset:2240\n\t" "s_add_u32 m0, m0, 32\n\t" "s_nop 0\n\t"
      "global_load_lds_dword %2, %3 offset:2464\n\t" "s_add_u32 m0, m0, 32\n\t" "s_nop 0\n\t"
      "global_load_lds_dword %2, %3 offset:2688\n\t" "s_add_u32 m0, m0, 32\n\t" "s_nop 0\n\t"
      "global_load_lds_dword %2, %3 offset:2912\n\t" "s_add_u32 m0, m0, 32\n\t" "s_nop 0\n\t"
      "global_load_lds_dword %2, %3 offset:3136\n\t" "s_add_u32 m0, m0, 32\n\t" "s_nop 0\n\t"
      "global_load_lds_dword %2, %3 offset:3360\n\t"
      "s_mov_b64 exec, %1\n\t"
      "s_mov_b32 m0, %0"
      : "=&s"(keep), "=&s"(ex)
      : "v"(voff), "s"(gbase), "s"(ldsdst)
      : "memory", "scc");
}

DI s16x4 vtr(LDSP(const char) p) {
  typedef short v4s __attribute__((ext_vector_type(4)));
  return __builtin_amdgcn_ds_read_tr16_b64_v4i16((LDSP(v4s))p);
}
DI bf16x8 vfrag(LDSP(const char) p) {
  s16x4 lo = vtr(p), hi = vtr(p + 512);
  return __builtin_bit_cast(bf16x8, __builtin_shufflevector(lo, hi, 0, 1, 2, 3, 4, 5, 6, 7));
}
DI f16x8 kfrag(LDSP(const char) p) { return *(LDSP(const f16x8))p; }

#define STAMP(i)
#define STAMP_DECL
#define STAMP_OUT

__global__ __launch_bounds__(512, 2) void qkn_attn(const char* __restrict__ q, const char* __restrict__ k, const char* __restrict__ v,
                                                   const unsigned* __restrict__ t, char* __restrict__ out, unsigned long long* ws) {
  extern __shared__ __attribute__((aligned(16))) char smem[];
  STAMP_DECL;
  STAMP(0);
  const int bid = blockIdx.x, x = bid & 7, y = bid >> 3;
  const int head = x * 8 + (y >> 2), q0 = (y & 3) * 512;
  const size_t hoff = (size_t)head * SEQ * ROWB;
  LDSP(char) lds = (LDSP(char))smem;
  const unsigned lds0 = (unsigned)(size_t)lds;
  const int tid = threadIdx.x, lane = tid & 63, r = lane & 31, h = lane >> 5;
  const int wid = __builtin_amdgcn_readfirstlane(tid >> 6);
  const unsigned tw = t[(head & (NHEAD - 1)) >> 1];
  const float c = __uint_as_float((head & 1) ? (tw & 0xffff0000u) : (tw << 16)) * LOG2E;
  const bool fast = fabsf(c) <= FAST_LIMIT;

  if (wid >= 4) {
    const int pw = wid - 4, ptid = tid - 256;
    const char* Kh = k + hoff + (size_t)ptid * ROWB;
    const char* Vh = v + hoff + (size_t)pw * 64 * ROWB;
    const unsigned voff = (unsigned)((lane >> 4) * ROWB + (lane & 15) * 4);
    const unsigned vdst = lds0 + pw * 2 * BLK + 2048;
    LDSP(char) kdst = lds + (ptid >> 5) * BLK + (ptid & 31) * 16;
    KRaw ka = k_load(Kh);
    KRaw kb = k_load(Kh + CHUNK * ROWB);
    v_dma(Vh, voff, vdst);
    {
      const u32x2 one = {0x00003F80u, 0u};
#pragma unroll
      for (int s = 0; s < NSTAGE; ++s) *(LDSP(u32x2))(lds + s * STAGE + (ptid >> 5) * BLK + 2048 + (ptid & 31) * 64 + 56) = one;
    }
    const unsigned kpad = fast ? pk_bf16(-fabsf(c), 0.f) : 0u;
    k_norm_store(ka, kdst, kpad);
    v_dma(Vh + CHUNK * ROWB, voff, vdst + STAGE);
    asm volatile("s_waitcnt vmcnt(16) lgkmcnt(0)\n\ts_barrier" ::: "memory");
#pragma unroll 1
    for (int ch = 1; ch < NCHUNK; ++ch) {
      __builtin_amdgcn_s_waitcnt(0x0F70);
      ka = kb;
      if (ch + 1 < NCHUNK) {
        kb = k_load(Kh + (size_t)(ch + 1) * CHUNK * ROWB);
        v_dma(Vh + (size_t)(ch + 1) * CHUNK * ROWB, voff, vdst + ((ch + 1) & (NSTAGE - 1)) * STAGE);
      }
      k_norm_store(ka, kdst + (ch & (NSTAGE - 1)) * STAGE, kpad);
      asm volatile("s_waitcnt lgkmcnt(0)\n\ts_barrier" ::: "memory");
    }
    return;
  }

  const char* Qw = q + hoff + (size_t)(q0 + wid * 128 + r) * ROWB + h * 16;
  u32x4 qa[4]; u32x2 qb[4], qc[4];
#pragma unroll
  for (int b = 0; b < 4; ++b) {
    const char* qp = Qw + b * 32 * ROWB;
    qa[b] = *(const u32x4a8*)qp;
    qb[b] = *(const u32x2*)(qp + 32);
    qc[b] = *(const u32x2*)(qp + (h ? 32 : 40));
  }
  u32x4 qf[4][2];
  const unsigned hmask = h ? 0u : 0xffffffffu;
  const float qscale = fast ? c : 1.0f;
  const unsigned qpad = h ? 0x00003F80u : 0u;
#pragma unroll
  for (int b = 0; b < 4; ++b) {
    qc[b][0] &= hmask; qc[b][1] &= hmask;
    float s0 = dot2(qa[b][0], 0.f), s1 = dot2(qa[b][1], 0.f);
    s0 = dot2(qa[b][2], s0); s1 = dot2(qa[b][3], s1);
    s0 = dot2(qb[b][0], s0); s1 = dot2(qb[b][1], s1);
    s0 = dot2(qc[b][0], s0); s1 = dot2(qc[b][1], s1);
    const float ss = swap_sum(s0 + s1);
    const float rq = qscale * __builtin_amdgcn_rcpf(__builtin_amdgcn_sqrtf(ss) + EPS);
    qf[b][0] = (u32x4){scale_pk(qa[b][0], rq), scale_pk(qa[b][1], rq), scale_pk(qa[b][2], rq), scale_pk(qa[b][3], rq)};
    qf[b][1] = (u32x4){scale_pk(qb[b][0], rq), scale_pk(qb[b][1], rq), scale_pk(qc[b][0], rq) | qpad, scale_pk(qc[b][1], rq)};
  }
  f32x16 o[4] = {};
  unsigned kl = lds0 + h * 512 + r * 16;
  unsigned vl = lds0 + 2048 + (4 * h + ((lane & 15) >> 2)) * 64 + ((lane >> 4) & 1) * 32 + (lane & 3) * 8;
  STAMP(1);
  if (fast) {
    unsigned cnt, cch;
    asm volatile(
      "s_setprio 3\n\t"
      "v_mov_b32 v232, 0\n\t"
      "v_mov_b32 v233, 0\n\t"
      "v_mov_b32 v234, 0\n\t"
      "v_mov_b32 v235, 0\n\t"
      "v_mov_b32 v236, 0\n\t"
      "v_mov_b32 v237, 0\n\t"
      "v_mov_b32 v238, 0\n\t"
      "v_mov_b32 v239, 0\n\t"
      "v_mov_b32 v184, 0\n\t"
      "v_mov_b32 v185, 0\n\t"
      "v_mov_b32 v186, 0\n\t"
      "v_mov_b32 v187, 0\n\t"
      "v_mov_b32 v188, 0\n\t"
      "v_mov_b32 v189, 0\n\t"
      "v_mov_b32 v190, 0\n\t"
      "v_mov_b32 v191, 0\n\t"
      "s_mov_b64 vcc, 0\n\t"
      "s_barrier\n\t"
      "ds_read_b128 v[240:243], %4 offset:0\n\t"
      "ds_read_b128 v[244:247], %4 offset:1024\n\t"
      "ds_read_b64_tr_b16 v[248:249], %5 offset:0\n\t"
      "ds_read_b64_tr_b16 v[250:251], %5 offset:512\n\t"
      "ds_read_b64_tr_b16 v[252:253], %5 offset:1024\n\t"
      "ds_read_b64_tr_b16 v[254:255], %5 offset:1536\n\t"
      "s_mov_b32 %6, 1\n\t"
      "s_mov_b32 %7, 8\n\t"
      "s_waitcnt lgkmcnt(4)\n\t"
      "v_mfma_f32_32x32x16_bf16 v[192:207], v[240:243], %8, 0\n\t"
      "v_mfma_f32_32x32x16_bf16 v[192:207], v[244:247], %9, v[192:207]\n\t"
      "s_waitcnt lgkmcnt(0)\n\t"
      "s_nop 10\n\t"
      "s_branch LMID%=\n\t"
      "LOUTER%=:\n\t"
      "s_barrier\n\t"
      "v_and_b32 %4, 0x1ffff, %4\n\t"
      "v_and_b32 %5, 0x1ffff, %5\n\t"
      "s_mov_b32 %6, 1\n\t"
      "LTOP%=:\n\t"
      "v_exp_f32 v192, v192\n\t"
      "v_exp_f32 v193, v193\n\t"
      "v_exp_f32 v194, v194\n\t"
      "v_exp_f32 v195, v195\n\t"
      "v_mfma_f32_32x32x16_bf16 v[208:223], v[240:243], %14, 0\n\t"
      "ds_read_b128 v[240:243], %4 offset:0\n\t"
      "ds_read_b64_tr_b16 v[248:249], %5 offset:0\n\t"
      "v_cvt_pk_bf16_f32 v224, v192, v193\n\t"
      "v_cvt_pk_bf16_f32 v225, v194, v195\n\t"
      "v_exp_f32 v196, v196\n\t"
      "v_exp_f32 v197, v197\n\t"
      "v_exp_f32 v198, v198\n\t"
      "v_exp_f32 v199, v199\n\t"
      "v_mfma_f32_32x32x16_bf16 v[208:223], v[244:247], %15, v[208:223]\n\t"
      "ds_read_b128 v[244:247], %4 offset:1024\n\t"
      "ds_read_b64_tr_b16 v[250:251], %5 offset:512\n\t"
      "v_cvt_pk_bf16_f32 v226, v196, v197\n\t"
      "v_cvt_pk_bf16_f32 v227, v198, v199\n\t"
      "v_exp_f32 v200, v200\n\t"
      "v_exp_f32 v201, v201\n\t"
      "v_exp_f32 v202, v202\n\t"
      "v_exp_f32 v203, v203\n\t"
      "v_mfma_f32_32x32x16_bf16 %1, v[184:187], v[232:235], %1\n\t"
      "ds_read_b64_tr_b16 v[252:253], %5 offset:1024\n\t"
      "v_cvt_pk_bf16_f32 v228, v200, v201\n\t"
      "v_cvt_pk_bf16_f32 v229, v202, v203\n\t"
      "v_exp_f32 v204, v204\n\t"
      "v_exp_f32 v205, v205\n\t"
      "v_exp_f32 v206, v206\n\t"
      "v_exp_f32 v207, v207\n\t"
      "v_mfma_f32_32x32x16_bf16 %1, v[188:191], v[236:239], %1\n\t"
      "ds_read_b64_tr_b16 v[254:255], %5 offset:1536\n\t"
      "v_cvt_pk_bf16_f32 v230, v204, v205\n\t"
      "v_cvt_pk_bf16_f32 v231, v206, v207\n\t"
      "v_exp_f32 v208, v208\n\t"
      "v_exp_f32 v209, v209\n\t"
      "v_exp_f32 v210, v210\n\t"
      "v_exp_f32 v211, v211\n\t"
      "s_waitcnt lgkmcnt(0)\n\t"
      "v_mfma_f32_32x32x16_bf16 v[192:207], v[240:243], %8, 0\n\t"
      "v_cvt_pk_bf16_f32 v232, v208, v209\n\t"
      "v_cvt_pk_bf16_f32 v233, v210, v211\n\t"
      "v_exp_f32 v212, v212\n\t"
      "v_exp_f32 v213, v213\n\t"
      "v_exp_f32 v214, v214\n\t"
      "v_exp_f32 v215, v215\n\t"
      "v_mfma_f32_32x32x16_bf16 v[192:207], v[244:247], %9, v[192:207]\n\t"
      "v_cvt_pk_bf16_f32 v234, v212, v213\n\t"
      "v_cvt_pk_bf16_f32 v235, v214, v215\n\t"
      "v_exp_f32 v216, v216\n\t"
      "v_exp_f32 v217, v217\n\t"
      "v_exp_f32 v218, v218\n\t"
      "v_exp_f32 v219, v219\n\t"
      "v_mfma_f32_32x32x16_bf16 %2, v[184:187], v[224:227], %2\n\t"
      "v_cvt_pk_bf16_f32 v236, v216, v217\n\t"
      "v_cvt_pk_bf16_f32 v237, v218, v219\n\t"
      "v_exp_f32 v220, v220\n\t"
      "v_exp_f32 v221, v221\n\t"
      "v_exp_f32 v222, v222\n\t"
      "v_exp_f32 v223, v223\n\t"
      "v_mfma_f32_32x32x16_bf16 %2, v[188:191], v[228:231], %2\n\t"
      "v_cvt_pk_bf16_f32 v238, v220, v221\n\t"
      "v_cvt_pk_bf16_f32 v239, v222, v223\n\t"
      "s_cbranch_vccnz LDONE%=\n\t"
      "LMID%=:\n\t"
      "v_exp_f32 v192, v192\n\t"
      "v_exp_f32 v193, v193\n\t"
      "v_exp_f32 v194, v194\n\t"
      "v_exp_f32 v195, v195\n\t"
      "v_mfma_f32_32x32x16_bf16 v[208:223], v[240:243], %10, 0\n\t"
      "v_cvt_pk_bf16_f32 v224, v192, v193\n\t"
      "v_cvt_pk_bf16_f32 v225, v194, v195\n\t"
      "v_exp_f32 v196, v196\n\t"
      "v_exp_f32 v197, v197\n\t"
      "v_exp_f32 v198, v198\n\t"
      "v_exp_f32 v199, v199\n\t"
      "v_mfma_f32_32x32x16_bf16 v[208:223], v[244:247], %11, v[208:223]\n\t"
      "v_cvt_pk_bf16_f32 v226, v196, v197\n\t"
      "v_cvt_pk_bf16_f32 v227, v198, v199\n\t"
      "v_exp_f32 v200, v200\n\t"
      "v_exp_f32 v201, v201\n\t"
      "v_exp_f32 v202, v202\n\t"
      "v_exp_f32 v203, v203\n\t"
      "v_mfma_f32_32x32x16_bf16 %3, v[184:187], v[232:235], %3\n\t"
      "v_cvt_pk_bf16_f32 v228, v200, v201\n\t"
      "v_cvt_pk_bf16_f32 v229, v202, v203\n\t"
      "v_exp_f32 v204, v204\n\t"
      "v_exp_f32 v205, v205\n\t"
      "v_exp_f32 v206, v206\n\t"
      "v_exp_f32 v207, v207\n\t"
      "v_mfma_f32_32x32x16_bf16 %3, v[188:191], v[236:239], %3\n\t"
      "v_cvt_pk_bf16_f32 v230, v204, v205\n\t"
      "v_cvt_pk_bf16_f32 v231, v206, v207\n\t"
      "v_exp_f32 v208, v208\n\t"
      "v_exp_f32 v209, v209\n\t"
      "v_exp_f32 v210, v210\n\t"
      "v_exp_f32 v211, v211\n\t"
      "v_mfma_f32_32x32x16_bf16 v[192:207], v[240:243], %12, 0\n\t"
      "v_cvt_pk_bf16_f32 v232, v208, v209\n\t"
      "v_cvt_pk_bf16_f32 v233, v210, v211\n\t"
      "v_exp_f32 v212, v212\n\t"
      "v_exp_f32 v213, v213\n\t"
      "v_exp_f32 v214, v214\n\t"
      "v_exp_f32 v215, v215\n\t"
      "v_mfma_f32_32x32x16_bf16 v[192:207], v[244:247], %13, v[192:207]\n\t"
      "v_cvt_pk_bf16_f32 v234, v212, v213\n\t"
      "v_cvt_pk_bf16_f32 v235, v214, v215\n\t"
      "v_exp_f32 v216, v216\n\t"
      "v_exp_f32 v217, v217\n\t"
      "v_exp_f32 v218, v218\n\t"
      "v_exp_f32 v219, v219\n\t"
      "v_mfma_f32_32x32x16_bf16 %0, v[248:251], v[224:227], %0\n\t"
      "v_cvt_pk_bf16_f32 v236, v216, v217\n\t"
      "v_cvt_pk_bf16_f32 v237, v218, v219\n\t"
      "v_exp_f32 v220, v220\n\t"
      "v_exp_f32 v221, v221\n\t"
      "v_exp_f32 v222, v222\n\t"
      "v_exp_f32 v223, v223\n\t"
      "v_mfma_f32_32x32x16_bf16 %0, v[252:255], v[228:231], %0\n\t"
      "v_cvt_pk_bf16_f32 v238, v220, v221\n\t"
      "v_cvt_pk_bf16_f32 v239, v222, v223\n\t"
      "v_exp_f32 v192, v192\n\t"
      "v_exp_f32 v193, v193\n\t"
      "v_exp_f32 v194, v194\n\t"
      "v_exp_f32 v195, v195\n\t"
      "v_mfma_f32_32x32x16_bf16 v[208:223], v[240:243], %14, 0\n\t"
      "ds_read_b128 v[240:243], %4 offset:4096\n\t"
      "ds_read_b64_tr_b16 v[184:185], %5 offset:4096\n\t"
      "v_cvt_pk_bf16_f32 v224, v192, v193\n\t"
      "v_cvt_pk_bf16_f32 v225, v194, v195\n\t"
      "v_exp_f32 v196, v196\n\t"
      "v_exp_f32 v197, v197\n\t"
      "v_exp_f32 v198, v198\n\t"
      "v_exp_f32 v199, v199\n\t"
      "v_mfma_f32_32x32x16_bf16 v[208:223], v[244:247], %15, v[208:223]\n\t"
      "ds_read_b128 v[244:247], %4 offset:5120\n\t"
      "ds_read_b64_tr_b16 v[186:187], %5 offset:4608\n\t"
      "v_cvt_pk_bf16_f32 v226, v196, v197\n\t"
      "v_cvt_pk_bf16_f32 v227, v198, v199\n\t"
      "v_exp_f32 v200, v200\n\t"
      "v_exp_f32 v201, v201\n\t"
      "v_exp_f32 v202, v202\n\t"
      "v_exp_f32 v203, v203\n\t"
      "v_mfma_f32_32x32x16_bf16 %1, v[248:251], v[232:235], %1\n\t"
      "ds_read_b64_tr_b16 v[188:189], %5 offset:5120\n\t"
      "v_cvt_pk_bf16_f32 v228, v200, v201\n\t"
      "v_cvt_pk_bf16_f32 v229, v202, v203\n\t"
      "v_exp_f32 v204, v204\n\t"
      "v_exp_f32 v205, v205\n\t"
      "v_exp_f32 v206, v206\n\t"
      "v_exp_f32 v207, v207\n\t"
      "v_mfma_f32_32x32x16_bf16 %1, v[252:255], v[236:239], %1\n\t"
      "ds_read_b64_tr_b16 v[190:191], %5 offset:5632\n\t"
      "v_cvt_pk_bf16_f32 v230, v204, v205\n\t"
      "v_cvt_pk_bf16_f32 v231, v206, v207\n\t"
      "v_exp_f32 v208, v208\n\t"
      "v_exp_f32 v209, v209\n\t"
      "v_exp_f32 v210, v210\n\t"
      "v_exp_f32 v211, v211\n\t"
      "s_waitcnt lgkmcnt(0)\n\t"
      "v_mfma_f32_32x32x16_bf16 v[192:207], v[240:243], %8, 0\n\t"
      "v_cvt_pk_bf16_f32 v232, v208, v209\n\t"
      "v_cvt_pk_bf16_f32 v233, v210, v211\n\t"
      "v_exp_f32 v212, v212\n\t"
      "v_exp_f32 v213, v213\n\t"
      "v_exp_f32 v214, v214\n\t"
      "v_exp_f32 v215, v215\n\t"
      "v_mfma_f32_32x32x16_bf16 v[192:207], v[244:247], %9, v[192:207]\n\t"
      "v_cvt_pk_bf16_f32 v234, v212, v213\n\t"
      "v_cvt_pk_bf16_f32 v235, v214, v215\n\t"
      "v_exp_f32 v216, v216\n\t"
      "v_exp_f32 v217, v217\n\t"
      "v_exp_f32 v218, v218\n\t"
      "v_exp_f32 v219, v219\n\t"
      "v_mfma_f32_32x32x16_bf16 %2, v[248:251], v[224:227], %2\n\t"
      "v_cvt_pk_bf16_f32 v236, v216, v217\n\t"
      "v_cvt_pk_bf16_f32 v237, v218, v219\n\t"
      "v_exp_f32 v220, v220\n\t"
      "v_exp_f32 v221, v221\n\t"
      "v_exp_f32 v222, v222\n\t"
      "v_exp_f32 v223, v223\n\t"
      "v_mfma_f32_32x32x16_bf16 %2, v[252:255], v[228:231], %2\n\t"
      "v_cvt_pk_bf16_f32 v238, v220, v221\n\t"
      "v_cvt_pk_bf16_f32 v239, v222, v223\n\t"
      "v_exp_f32 v192, v192\n\t"
      "v_exp_f32 v193, v193\n\t"
      "v_exp_f32 v194, v194\n\t"
      "v_exp_f32 v195, v195\n\t"
      "v_mfma_f32_32x32x16_bf16 v[208:223], v[240:243], %10, 0\n\t"
      "v_cvt_pk_bf16_f32 v224, v192, v193\n\t"
      "v_cvt_pk_bf16_f32 v225, v194, v195\n\t"
      "v_exp_f32 v196, v196\n\t"
      "v_exp_f32 v197, v197\n\t"
      "v_exp_f32 v198, v198\n\t"
      "v_exp_f32 v199, v199\n\t"
      "v_mfma_f32_32x32x16_bf16 v[208:223], v[244:247], %11, v[208:223]\n\t"
      "v_cvt_pk_bf16_f32 v226, v196, v197\n\t"
      "v_cvt_pk_bf16_f32 v227, v198, v199\n\t"
      "v_exp_f32 v200, v200\n\t"
      "v_exp_f32 v201, v201\n\t"
      "v_exp_f32 v202, v202\n\t"
      "v_exp_f32 v203, v203\n\t"
      "v_mfma_f32_32x32x16_bf16 %3, v[248:251], v[232:235], %3\n\t"
      "v_cvt_pk_bf16_f32 v228, v200, v201\n\t"
      "v_cvt_pk_bf16_f32 v229, v202, v203\n\t"
      "v_exp_f32 v204, v204\n\t"
      "v_exp_f32 v205, v205\n\t"
      "v_exp_f32 v206, v206\n\t"
      "v_exp_f32 v207, v207\n\t"
      "v_mfma_f32_32x32x16_bf16 %3, v[252:255], v[236:239], %3\n\t"
      "v_cvt_pk_bf16_f32 v230, v204, v205\n\t"
      "v_cvt_pk_bf16_f32 v231, v206, v207\n\t"
      "v_exp_f32 v208, v208\n\t"
      "v_exp_f32 v209, v209\n\t"
      "v_exp_f32 v210, v210\n\t"
      "v_exp_f32 v211, v211\n\t"
      "v_mfma_f32_32x32x16_bf16 v[192:207], v[240:243], %12, 0\n\t"
      "v_cvt_pk_bf16_f32 v232, v208, v209\n\t"
      "v_cvt_pk_bf16_f32 v233, v210, v211\n\t"
      "v_exp_f32 v212, v212\n\t"
      "v_exp_f32 v213, v213\n\t"
      "v_exp_f32 v214, v214\n\t"
      "v_exp_f32 v215, v215\n\t"
      "v_mfma_f32_32x32x16_bf16 v[192:207], v[244:247], %13, v[192:207]\n\t"
      "v_cvt_pk_bf16_f32 v234, v212, v213\n\t"
      "v_cvt_pk_bf16_f32 v235, v214, v215\n\t"
      "v_exp_f32 v216, v216\n\t"
      "v_exp_f32 v217, v217\n\t"
      "v_exp_f32 v218, v218\n\t"
      "v_exp_f32 v219, v219\n\t"
      "v_mfma_f32_32x32x16_bf16 %0, v[184:187], v[224:227], %0\n\t"
      "v_cvt_pk_bf16_f32 v236, v216, v217\n\t"
      "v_cvt_pk_bf16_f32 v237, v218, v219\n\t"
      "v_exp_f32 v220, v220\n\t"
      "v_exp_f32 v221, v221\n\t"
      "v_exp_f32 v222, v222\n\t"
      "v_exp_f32 v223, v223\n\t"
      "v_mfma_f32_32x32x16_bf16 %0, v[188:191], v[228:231], %0\n\t"
      "v_cvt_pk_bf16_f32 v238, v220, v221\n\t"
      "v_cvt_pk_bf16_f32 v239, v222, v223\n\t"
      "v_exp_f32 v192, v192\n\t"
      "v_exp_f32 v193, v193\n\t"
      "v_exp_f32 v194, v194\n\t"
      "v_exp_f32 v195, v195\n\t"
      "v_mfma_f32_32x32x16_bf16 v[208:223], v[240:243], %14, 0\n\t"
      "ds_read_b128 v[240:243], %4 offset:8192\n\t"
      "ds_read_b64_tr_b16 v[248:249], %5 offset:8192\n\t"
      "v_cvt_pk_bf16_f32 v224, v192, v193\n\t"
      "v_cvt_pk_bf16_f32 v225, v194, v195\n\t"
      "v_exp_f32 v196, v196\n\t"
      "v_exp_f32 v197, v197\n\t"
      "v_exp_f32 v198, v198\n\t"
      "v_exp_f32 v199, v199\n\t"
      "v_mfma_f32_32x32x16_bf16 v[208:223], v[244:247], %15, v[208:223]\n\t"
      "ds_read_b128 v[244:247], %4 offset:9216\n\t"
      "ds_read_b64_tr_b16 v[250:251], %5 offset:8704\n\t"
      "v_cvt_pk_bf16_f32 v226, v196, v197\n\t"
      "v_cvt_pk_bf16_f32 v227, v198, v199\n\t"
      "v_exp_f32 v200, v200\n\t"
      "v_exp_f32 v201, v201\n\t"
      "v_exp_f32 v202, v202\n\t"
      "v_exp_f32 v203, v203\n\t"
      "v_mfma_f32_32x32x16_bf16 %1, v[184:187], v[232:235], %1\n\t"
      "ds_read_b64_tr_b16 v[252:253], %5 offset:9216\n\t"
      "v_cvt_pk_bf16_f32 v228, v200, v201\n\t"
      "v_cvt_pk_bf16_f32 v229, v202, v203\n\t"
      "v_exp_f32 v204, v204\n\t"
      "v_exp_f32 v205, v205\n\t"
      "v_exp_f32 v206, v206\n\t"
      "v_exp_f32 v207, v207\n\t"
      "v_mfma_f32_32x32x16_bf16 %1, v[188:191], v[236:239], %1\n\t"
      "ds_read_b64_tr_b16 v[254:255], %5 offset:9728\n\t"
      "v_cvt_pk_bf16_f32 v230, v204, v205\n\t"
      "v_cvt_pk_bf16_f32 v231, v206, v207\n\t"
      "v_exp_f32 v208, v208\n\t"
      "v_exp_f32 v209, v209\n\t"
      "v_exp_f32 v210, v210\n\t"
      "v_exp_f32 v211, v211\n\t"
      "s_waitcnt lgkmcnt(0)\n\t"
      "v_mfma_f32_32x32x16_bf16 v[192:207], v[240:243], %8, 0\n\t"
      "v_cvt_pk_bf16_f32 v232, v208, v209\n\t"
      "v_cvt_pk_bf16_f32 v233, v210, v211\n\t"
      "v_exp_f32 v212, v212\n\t"
      "v_exp_f32 v213, v213\n\t"
      "v_exp_f32 v214, v214\n\t"
      "v_exp_f32 v215, v215\n\t"
      "v_mfma_f32_32x32x16_bf16 v[192:207], v[244:247], %9, v[192:207]\n\t"
      "v_cvt_pk_bf16_f32 v234, v212, v213\n\t"
      "v_cvt_pk_bf16_f32 v235, v214, v215\n\t"
      "v_exp_f32 v216, v216\n\t"
      "v_exp_f32 v217, v217\n\t"
      "v_exp_f32 v218, v218\n\t"
      "v_exp_f32 v219, v219\n\t"
      "v_mfma_f32_32x32x16_bf16 %2, v[184:187], v[224:227], %2\n\t"
      "v_cvt_pk_bf16_f32 v236, v216, v217\n\t"
      "v_cvt_pk_bf16_f32 v237, v218, v219\n\t"
      "v_exp_f32 v220, v220\n\t"
      "v_exp_f32 v221, v221\n\t"
      "v_exp_f32 v222, v222\n\t"
      "v_exp_f32 v223, v223\n\t"
      "v_mfma_f32_32x32x16_bf16 %2, v[188:191], v[228:231], %2\n\t"
      "v_cvt_pk_bf16_f32 v238, v220, v221\n\t"
      "v_cvt_pk_bf16_f32 v239, v222, v223\n\t"
      "v_exp_f32 v192, v192\n\t"
      "v_exp_f32 v193, v193\n\t"
      "v_exp_f32 v194, v194\n\t"
      "v_exp_f32 v195, v195\n\t"
      "v_mfma_f32_32x32x16_bf16 v[208:223], v[240:243], %10, 0\n\t"
      "v_cvt_pk_bf16_f32 v224, v192, v193\n\t"
      "v_cvt_pk_bf16_f32 v225, v194, v195\n\t"
      "v_exp_f32 v196, v196\n\t"
      "v_exp_f32 v197, v197\n\t"
      "v_exp_f32 v198, v198\n\t"
      "v_exp_f32 v199, v199\n\t"
      "v_mfma_f32_32x32x16_bf16 v[208:223], v[244:247], %11, v[208:223]\n\t"
      "v_cvt_pk_bf16_f32 v226, v196, v197\n\t"
      "v_cvt_pk_bf16_f32 v227, v198, v199\n\t"
      "v_exp_f32 v200, v200\n\t"
      "v_exp_f32 v201, v201\n\t"
      "v_exp_f32 v202, v202\n\t"
      "v_exp_f32 v203, v203\n\t"
      "v_mfma_f32_32x32x16_bf16 %3, v[184:187], v[232:235], %3\n\t"
      "v_cvt_pk_bf16_f32 v228, v200, v201\n\t"
      "v_cvt_pk_bf16_f32 v229, v202, v203\n\t"
      "v_exp_f32 v204, v204\n\t"
      "v_exp_f32 v205, v205\n\t"
      "v_exp_f32 v206, v206\n\t"
      "v_exp_f32 v207, v207\n\t"
      "v_mfma_f32_32x32x16_bf16 %3, v[188:191], v[236:239], %3\n\t"
      "v_cvt_pk_bf16_f32 v230, v204, v205\n\t"
      "v_cvt_pk_bf16_f32 v231, v206, v207\n\t"
      "v_exp_f32 v208, v208\n\t"
      "v_exp_f32 v209, v209\n\t"
      "v_exp_f32 v210, v210\n\t"
      "v_exp_f32 v211, v211\n\t"
      "v_mfma_f32_32x32x16_bf16 v[192:207], v[240:243], %12, 0\n\t"
      "v_cvt_pk_bf16_f32 v232, v208, v209\n\t"
      "v_cvt_pk_bf16_f32 v233, v210, v211\n\t"
      "v_exp_f32 v212, v212\n\t"
      "v_exp_f32 v213, v213\n\t"
      "v_exp_f32 v214, v214\n\t"
      "v_exp_f32 v215, v215\n\t"
      "v_mfma_f32_32x32x16_bf16 v[192:207], v[244:247], %13, v[192:207]\n\t"
      "v_cvt_pk_bf16_f32 v234, v212, v213\n\t"
      "v_cvt_pk_bf16_f32 v235, v214, v215\n\t"
      "v_exp_f32 v216, v216\n\t"
      "v_exp_f32 v217, v217\n\t"
      "v_exp_f32 v218, v218\n\t"
      "v_exp_f32 v219, v219\n\t"
      "v_mfma_f32_32x32x16_bf16 %0, v[248:251], v[224:227], %0\n\t"
      "v_cvt_pk_bf16_f32 v236, v216, v217\n\t"
      "v_cvt_pk_bf16_f32 v237, v218, v219\n\t"
      "v_exp_f32 v220, v220\n\t"
      "v_exp_f32 v221, v221\n\t"
      "v_exp_f32 v222, v222\n\t"
      "v_exp_f32 v223, v223\n\t"
      "v_mfma_f32_32x32x16_bf16 %0, v[252:255], v[228:231], %0\n\t"
      "v_cvt_pk_bf16_f32 v238, v220, v221\n\t"
      "v_cvt_pk_bf16_f32 v239, v222, v223\n\t"
      "v_exp_f32 v192, v192\n\t"
      "v_exp_f32 v193, v193\n\t"
      "v_exp_f32 v194, v194\n\t"
      "v_exp_f32 v195, v195\n\t"
      "v_mfma_f32_32x32x16_bf16 v[208:223], v[240:243], %14, 0\n\t"
      "ds_read_b128 v[240:243], %4 offset:12288\n\t"
      "ds_read_b64_tr_b16 v[184:185], %5 offset:12288\n\t"
      "v_cvt_pk_bf16_f32 v224, v192, v193\n\t"
      "v_cvt_pk_bf16_f32 v225, v194, v195\n\t"
      "v_exp_f32 v196, v196\n\t"
      "v_exp_f32 v197, v197\n\t"
      "v_exp_f32 v198, v198\n\t"
      "v_exp_f32 v199, v199\n\t"
      "v_mfma_f32_32x32x16_bf16 v[208:223], v[244:247], %15, v[208:223]\n\t"
      "ds_read_b128 v[244:247], %4 offset:13312\n\t"
      "ds_read_b64_tr_b16 v[186:187], %5 offset:12800\n\t"
      "v_cvt_pk_bf16_f32 v226, v196, v197\n\t"
      "v_cvt_pk_bf16_f32 v227, v198, v199\n\t"
      "v_exp_f32 v200, v200\n\t"
      "v_exp_f32 v201, v201\n\t"
      "v_exp_f32 v202, v202\n\t"
      "v_exp_f32 v203, v203\n\t"
      "v_mfma_f32_32x32x16_bf16 %1, v[248:251], v[232:235], %1\n\t"
      "ds_read_b64_tr_b16 v[188:189], %5 offset:13312\n\t"
      "v_cvt_pk_bf16_f32 v228, v200, v201\n\t"
      "v_cvt_pk_bf16_f32 v229, v202, v203\n\t"
      "v_exp_f32 v204, v204\n\t"
      "v_exp_f32 v205, v205\n\t"
      "v_exp_f32 v206, v206\n\t"
      "v_exp_f32 v207, v207\n\t"
      "v_mfma_f32_32x32x16_bf16 %1, v[252:255], v[236:239], %1\n\t"
      "ds_read_b64_tr_b16 v[190:191], %5 offset:13824\n\t"
      "v_cvt_pk_bf16_f32 v230, v204, v205\n\t"
      "v_cvt_pk_bf16_f32 v231, v206, v207\n\t"
      "v_exp_f32 v208, v208\n\t"
      "v_exp_f32 v209, v209\n\t"
      "v_exp_f32 v210, v210\n\t"
      "v_exp_f32 v211, v211\n\t"
      "s_waitcnt lgkmcnt(0)\n\t"
      "v_mfma_f32_32x32x16_bf16 v[192:207], v[240:243], %8, 0\n\t"
      "v_cvt_pk_bf16_f32 v232, v208, v209\n\t"
      "v_cvt_pk_bf16_f32 v233, v210, v211\n\t"
      "v_exp_f32 v212, v212\n\t"
      "v_exp_f32 v213, v213\n\t"
      "v_exp_f32 v214, v214\n\t"
      "v_exp_f32 v215, v215\n\t"
      "v_mfma_f32_32x32x16_bf16 v[192:207], v[244:247], %9, v[192:207]\n\t"
      "v_cvt_pk_bf16_f32 v234, v212, v213\n\t"
      "v_cvt_pk_bf16_f32 v235, v214, v215\n\t"
      "v_exp_f32 v216, v216\n\t"
      "v_exp_f32 v217, v217\n\t"
      "v_exp_f32 v218, v218\n\t"
      "v_exp_f32 v219, v219\n\t"
      "v_mfma_f32_32x32x16_bf16 %2, v[248:251], v[224:227], %2\n\t"
      "v_cvt_pk_bf16_f32 v236, v216, v217\n\t"
      "v_cvt_pk_bf16_f32 v237, v218, v219\n\t"
      "v_exp_f32 v220, v220\n\t"
      "v_exp_f32 v221, v221\n\t"
      "v_exp_f32 v222, v222\n\t"
      "v_exp_f32 v223, v223\n\t"
      "v_mfma_f32_32x32x16_bf16 %2, v[252:255], v[228:231], %2\n\t"
      "v_cvt_pk_bf16_f32 v238, v220, v221\n\t"
      "v_cvt_pk_bf16_f32 v239, v222, v223\n\t"
      "v_exp_f32 v192, v192\n\t"
      "v_exp_f32 v193, v193\n\t"
      "v_exp_f32 v194, v194\n\t"
      "v_exp_f32 v195, v195\n\t"
      "v_mfma_f32_32x32x16_bf16 v[208:223], v[240:243], %10, 0\n\t"
      "v_cvt_pk_bf16_f32 v224, v192, v193\n\t"
      "v_cvt_pk_bf16_f32 v225, v194, v195\n\t"
      "v_exp_f32 v196, v196\n\t"
      "v_exp_f32 v197, v197\n\t"
      "v_exp_f32 v198, v198\n\t"
      "v_exp_f32 v199, v199\n\t"
      "v_mfma_f32_32x32x16_bf16 v[208:223], v[244:247], %11, v[208:223]\n\t"
      "v_cvt_pk_bf16_f32 v226, v196, v197\n\t"
      "v_cvt_pk_bf16_f32 v227, v198, v199\n\t"
      "v_exp_f32 v200, v200\n\t"
      "v_exp_f32 v201, v201\n\t"
      "v_exp_f32 v202, v202\n\t"
      "v_exp_f32 v203, v203\n\t"
      "v_mfma_f32_32x32x16_bf16 %3, v[248:251], v[232:235], %3\n\t"
      "v_cvt_pk_bf16_f32 v228, v200, v201\n\t"
      "v_cvt_pk_bf16_f32 v229, v202, v203\n\t"
      "v_exp_f32 v204, v204\n\t"
      "v_exp_f32 v205, v205\n\t"
      "v_exp_f32 v206, v206\n\t"
      "v_exp_f32 v207, v207\n\t"
      "v_mfma_f32_32x32x16_bf16 %3, v[252:255], v[236:239], %3\n\t"
      "v_cvt_pk_bf16_f32 v230, v204, v205\n\t"
      "v_cvt_pk_bf16_f32 v231, v206, v207\n\t"
      "v_exp_f32 v208, v208\n\t"
      "v_exp_f32 v209, v209\n\t"
      "v_exp_f32 v210, v210\n\t"
      "v_exp_f32 v211, v211\n\t"
      "v_mfma_f32_32x32x16_bf16 v[192:207], v[240:243], %12, 0\n\t"
      "v_cvt_pk_bf16_f32 v232, v208, v209\n\t"
      "v_cvt_pk_bf16_f32 v233, v210, v211\n\t"
      "v_exp_f32 v212, v212\n\t"
      "v_exp_f32 v213, v213\n\t"
      "v_exp_f32 v214, v214\n\t"
      "v_exp_f32 v215, v215\n\t"
      "v_mfma_f32_32x32x16_bf16 v[192:207], v[244:247], %13, v[192:207]\n\t"
      "v_cvt_pk_bf16_f32 v234, v212, v213\n\t"
      "v_cvt_pk_bf16_f32 v235, v214, v215\n\t"
      "v_exp_f32 v216, v216\n\t"
      "v_exp_f32 v217, v217\n\t"
      "v_exp_f32 v218, v218\n\t"
      "v_exp_f32 v219, v219\n\t"
      "v_mfma_f32_32x32x16_bf16 %0, v[184:187], v[224:227], %0\n\t"
      "v_cvt_pk_bf16_f32 v236, v216, v217\n\t"
      "v_cvt_pk_bf16_f32 v237, v218, v219\n\t"
      "v_exp_f32 v220, v220\n\t"
      "v_exp_f32 v221, v221\n\t"
      "v_exp_f32 v222, v222\n\t"
      "v_exp_f32 v223, v223\n\t"
      "v_mfma_f32_32x32x16_bf16 %0, v[188:191], v[228:231], %0\n\t"
      "v_cvt_pk_bf16_f32 v238, v220, v221\n\t"
      "v_cvt_pk_bf16_f32 v239, v222, v223\n\t"
      "v_exp_f32 v192, v192\n\t"
      "v_exp_f32 v193, v193\n\t"
      "v_exp_f32 v194, v194\n\t"
      "v_exp_f32 v195, v195\n\t"
      "v_mfma_f32_32x32x16_bf16 v[208:223], v[240:243], %14, 0\n\t"
      "ds_read_b128 v[240:243], %4 offset:16384\n\t"
      "ds_read_b64_tr_b16 v[248:249], %5 offset:16384\n\t"
      "v_cvt_pk_bf16_f32 v224, v192, v193\n\t"
      "v_cvt_pk_bf16_f32 v225, v194, v195\n\t"
      "v_exp_f32 v196, v196\n\t"
      "v_exp_f32 v197, v197\n\t"
      "v_exp_f32 v198, v198\n\t"
      "v_exp_f32 v199, v199\n\t"
      "v_mfma_f32_32x32x16_bf16 v[208:223], v[244:247], %15, v[208:223]\n\t"
      "ds_read_b128 v[244:247], %4 offset:17408\n\t"
      "ds_read_b64_tr_b16 v[250:251], %5 offset:16896\n\t"
      "v_cvt_pk_bf16_f32 v226, v196, v197\n\t"
      "v_cvt_pk_bf16_f32 v227, v198, v199\n\t"
      "v_exp_f32 v200, v200\n\t"
      "v_exp_f32 v201, v201\n\t"
      "v_exp_f32 v202, v202\n\t"
      "v_exp_f32 v203, v203\n\t"
      "v_mfma_f32_32x32x16_bf16 %1, v[184:187], v[232:235], %1\n\t"
      "ds_read_b64_tr_b16 v[252:253], %5 offset:17408\n\t"
      "v_cvt_pk_bf16_f32 v228, v200, v201\n\t"
      "v_cvt_pk_bf16_f32 v229, v202, v203\n\t"
      "v_exp_f32 v204, v204\n\t"
      "v_exp_f32 v205, v205\n\t"
      "v_exp_f32 v206, v206\n\t"
      "v_exp_f32 v207, v207\n\t"
      "v_mfma_f32_32x32x16_bf16 %1, v[188:191], v[236:239], %1\n\t"
      "ds_read_b64_tr_b16 v[254:255], %5 offset:17920\n\t"
      "v_cvt_pk_bf16_f32 v230, v204, v205\n\t"
      "v_cvt_pk_bf16_f32 v231, v206, v207\n\t"
      "v_exp_f32 v208, v208\n\t"
      "v_exp_f32 v209, v209\n\t"
      "v_exp_f32 v210, v210\n\t"
      "v_exp_f32 v211, v211\n\t"
      "s_waitcnt lgkmcnt(0)\n\t"
      "v_mfma_f32_32x32x16_bf16 v[192:207], v[240:243], %8, 0\n\t"
      "v_cvt_pk_bf16_f32 v232, v208, v209\n\t"
      "v_cvt_pk_bf16_f32 v233, v210, v211\n\t"
      "v_exp_f32 v212, v212\n\t"
      "v_exp_f32 v213, v213\n\t"
      "v_exp_f32 v214, v214\n\t"
      "v_exp_f32 v215, v215\n\t"
      "v_mfma_f32_32x32x16_bf16 v[192:207], v[244:247], %9, v[192:207]\n\t"
      "v_cvt_pk_bf16_f32 v234, v212, v213\n\t"
      "v_cvt_pk_bf16_f32 v235, v214, v215\n\t"
      "v_exp_f32 v216, v216\n\t"
      "v_exp_f32 v217, v217\n\t"
      "v_exp_f32 v218, v218\n\t"
      "v_exp_f32 v219, v219\n\t"
      "v_mfma_f32_32x32x16_bf16 %2, v[184:187], v[224:227], %2\n\t"
      "v_cvt_pk_bf16_f32 v236, v216, v217\n\t"
      "v_cvt_pk_bf16_f32 v237, v218, v219\n\t"
      "v_exp_f32 v220, v220\n\t"
      "v_exp_f32 v221, v221\n\t"
      "v_exp_f32 v222, v222\n\t"
      "v_exp_f32 v223, v223\n\t"
      "v_mfma_f32_32x32x16_bf16 %2, v[188:191], v[228:231], %2\n\t"
      "v_cvt_pk_bf16_f32 v238, v220, v221\n\t"
      "v_cvt_pk_bf16_f32 v239, v222, v223\n\t"
      "v_exp_f32 v192, v192\n\t"
      "v_exp_f32 v193, v193\n\t"
      "v_exp_f32 v194, v194\n\t"
      "v_exp_f32 v195, v195\n\t"
      "v_mfma_f32_32x32x16_bf16 v[208:223], v[240:243], %10, 0\n\t"
      "v_cvt_pk_bf16_f32 v224, v192, v193\n\t"
      "v_cvt_pk_bf16_f32 v225, v194, v195\n\t"
      "v_exp_f32 v196, v196\n\t"
      "v_exp_f32 v197, v197\n\t"
      "v_exp_f32 v198, v198\n\t"
      "v_exp_f32 v199, v199\n\t"
      "v_mfma_f32_32x32x16_bf16 v[208:223], v[244:247], %11, v[208:223]\n\t"
      "v_cvt_pk_bf16_f32 v226, v196, v197\n\t"
      "v_cvt_pk_bf16_f32 v227, v198, v199\n\t"
      "v_exp_f32 v200, v200\n\t"
      "v_exp_f32 v201, v201\n\t"
      "v_exp_f32 v202, v202\n\t"
      "v_exp_f32 v203, v203\n\t"
      "v_mfma_f32_32x32x16_bf16 %3, v[184:187], v[232:235], %3\n\t"
      "v_cvt_pk_bf16_f32 v228, v200, v201\n\t"
      "v_cvt_pk_bf16_f32 v229, v202, v203\n\t"
      "v_exp_f32 v204, v204\n\t"
      "v_exp_f32 v205, v205\n\t"
      "v_exp_f32 v206, v206\n\t"
      "v_exp_f32 v207, v207\n\t"
      "v_mfma_f32_32x32x16_bf16 %3, v[188:191], v[236:239], %3\n\t"
      "v_cvt_pk_bf16_f32 v230, v204, v205\n\t"
      "v_cvt_pk_bf16_f32 v231, v206, v207\n\t"
      "v_exp_f32 v208, v208\n\t"
      "v_exp_f32 v209, v209\n\t"
      "v_exp_f32 v210, v210\n\t"
      "v_exp_f32 v211, v211\n\t"
      "v_mfma_f32_32x32x16_bf16 v[192:207], v[240:243], %12, 0\n\t"
      "v_cvt_pk_bf16_f32 v232, v208, v209\n\t"
      "v_cvt_pk_bf16_f32 v233, v210, v211\n\t"
      "v_exp_f32 v212, v212\n\t"
      "v_exp_f32 v213, v213\n\t"
      "v_exp_f32 v214, v214\n\t"
      "v_exp_f32 v215, v215\n\t"
      "v_mfma_f32_32x32x16_bf16 v[192:207], v[244:247], %13, v[192:207]\n\t"
      "v_cvt_pk_bf16_f32 v234, v212, v213\n\t"
      "v_cvt_pk_bf16_f32 v235, v214, v215\n\t"
      "v_exp_f32 v216, v216\n\t"
      "v_exp_f32 v217, v217\n\t"
      "v_exp_f32 v218, v218\n\t"
      "v_exp_f32 v219, v219\n\t"
      "v_mfma_f32_32x32x16_bf16 %0, v[248:251], v[224:227], %0\n\t"
      "v_cvt_pk_bf16_f32 v236, v216, v217\n\t"
      "v_cvt_pk_bf16_f32 v237, v218, v219\n\t"
      "v_exp_f32 v220, v220\n\t"
      "v_exp_f32 v221, v221\n\t"
      "v_exp_f32 v222, v222\n\t"
      "v_exp_f32 v223, v223\n\t"
      "v_mfma_f32_32x32x16_bf16 %0, v[252:255], v[228:231], %0\n\t"
      "v_cvt_pk_bf16_f32 v238, v220, v221\n\t"
      "v_cvt_pk_bf16_f32 v239, v222, v223\n\t"
      "v_exp_f32 v192, v192\n\t"
      "v_exp_f32 v193, v193\n\t"
      "v_exp_f32 v194, v194\n\t"
      "v_exp_f32 v195, v195\n\t"
      "v_mfma_f32_32x32x16_bf16 v[208:223], v[240:243], %14, 0\n\t"
      "ds_read_b128 v[240:243], %4 offset:20480\n\t"
      "ds_read_b64_tr_b16 v[184:185], %5 offset:20480\n\t"
      "v_cvt_pk_bf16_f32 v224, v192, v193\n\t"
      "v_cvt_pk_bf16_f32 v225, v194, v195\n\t"
      "v_exp_f32 v196, v196\n\t"
      "v_exp_f32 v197, v197\n\t"
      "v_exp_f32 v198, v198\n\t"
      "v_exp_f32 v199, v199\n\t"
      "v_mfma_f32_32x32x16_bf16 v[208:223], v[244:247], %15, v[208:223]\n\t"
      "ds_read_b128 v[244:247], %4 offset:21504\n\t"
      "ds_read_b64_tr_b16 v[186:187], %5 offset:20992\n\t"
      "v_cvt_pk_bf16_f32 v226, v196, v197\n\t"
      "v_cvt_pk_bf16_f32 v227, v198, v199\n\t"
      "v_exp_f32 v200, v200\n\t"
      "v_exp_f32 v201, v201\n\t"
      "v_exp_f32 v202, v202\n\t"
      "v_exp_f32 v203, v203\n\t"
      "v_mfma_f32_32x32x16_bf16 %1, v[248:251], v[232:235], %1\n\t"
      "ds_read_b64_tr_b16 v[188:189], %5 offset:21504\n\t"
      "v_cvt_pk_bf16_f32 v228, v200, v201\n\t"
      "v_cvt_pk_bf16_f32 v229, v202, v203\n\t"
      "v_exp_f32 v204, v204\n\t"
      "v_exp_f32 v205, v205\n\t"
      "v_exp_f32 v206, v206\n\t"
      "v_exp_f32 v207, v207\n\t"
      "v_mfma_f32_32x32x16_bf16 %1, v[252:255], v[236:239], %1\n\t"
      "ds_read_b64_tr_b16 v[190:191], %5 offset:22016\n\t"
      "v_cvt_pk_bf16_f32 v230, v204, v205\n\t"
      "v_cvt_pk_bf16_f32 v231, v206, v207\n\t"
      "v_exp_f32 v208, v208\n\t"
      "v_exp_f32 v209, v209\n\t"
      "v_exp_f32 v210, v210\n\t"
      "v_exp_f32 v211, v211\n\t"
      "s_waitcnt lgkmcnt(0)\n\t"
      "v_mfma_f32_32x32x16_bf16 v[192:207], v[240:243], %8, 0\n\t"
      "v_cvt_pk_bf16_f32 v232, v208, v209\n\t"
      "v_cvt_pk_bf16_f32 v233, v210, v211\n\t"
      "v_exp_f32 v212, v212\n\t"
      "v_exp_f32 v213, v213\n\t"
      "v_exp_f32 v214, v214\n\t"
      "v_exp_f32 v215, v215\n\t"
      "v_mfma_f32_32x32x16_bf16 v[192:207], v[244:247], %9, v[192:207]\n\t"
      "v_cvt_pk_bf16_f32 v234, v212, v213\n\t"
      "v_cvt_pk_bf16_f32 v235, v214, v215\n\t"
      "v_exp_f32 v216, v216\n\t"
      "v_exp_f32 v217, v217\n\t"
      "v_exp_f32 v218, v218\n\t"
      "v_exp_f32 v219, v219\n\t"
      "v_mfma_f32_32x32x16_bf16 %2, v[248:251], v[224:227], %2\n\t"
      "v_cvt_pk_bf16_f32 v236, v216, v217\n\t"
      "v_cvt_pk_bf16_f32 v237, v218, v219\n\t"
      "v_exp_f32 v220, v220\n\t"
      "v_exp_f32 v221, v221\n\t"
      "v_exp_f32 v222, v222\n\t"
      "v_exp_f32 v223, v223\n\t"
      "v_mfma_f32_32x32x16_bf16 %2, v[252:255], v[228:231], %2\n\t"
      "v_cvt_pk_bf16_f32 v238, v220, v221\n\t"
      "v_cvt_pk_bf16_f32 v239, v222, v223\n\t"
      "v_exp_f32 v192, v192\n\t"
      "v_exp_f32 v193, v193\n\t"
      "v_exp_f32 v194, v194\n\t"
      "v_exp_f32 v195, v195\n\t"
      "v_mfma_f32_32x32x16_bf16 v[208:223], v[240:243], %10, 0\n\t"
      "v_cvt_pk_bf16_f32 v224, v192, v193\n\t"
      "v_cvt_pk_bf16_f32 v225, v194, v195\n\t"
      "v_exp_f32 v196, v196\n\t"
      "v_exp_f32 v197, v197\n\t"
      "v_exp_f32 v198, v198\n\t"
      "v_exp_f32 v199, v199\n\t"
      "v_mfma_f32_32x32x16_bf16 v[208:223], v[244:247], %11, v[208:223]\n\t"
      "v_cvt_pk_bf16_f32 v226, v196, v197\n\t"
      "v_cvt_pk_bf16_f32 v227, v198, v199\n\t"
      "v_exp_f32 v200, v200\n\t"
      "v_exp_f32 v201, v201\n\t"
      "v_exp_f32 v202, v202\n\t"
      "v_exp_f32 v203, v203\n\t"
      "v_mfma_f32_32x32x16_bf16 %3, v[248:251], v[232:235], %3\n\t"
      "v_cvt_pk_bf16_f32 v228, v200, v201\n\t"
      "v_cvt_pk_bf16_f32 v229, v202, v203\n\t"
      "v_exp_f32 v204, v204\n\t"
      "v_exp_f32 v205, v205\n\t"
      "v_exp_f32 v206, v206\n\t"
      "v_exp_f32 v207, v207\n\t"
      "v_mfma_f32_32x32x16_bf16 %3, v[252:255], v[236:239], %3\n\t"
      "v_cvt_pk_bf16_f32 v230, v204, v205\n\t"
      "v_cvt_pk_bf16_f32 v231, v206, v207\n\t"
      "v_exp_f32 v208, v208\n\t"
      "v_exp_f32 v209, v209\n\t"
      "v_exp_f32 v210, v210\n\t"
      "v_exp_f32 v211, v211\n\t"
      "v_mfma_f32_32x32x16_bf16 v[192:207], v[240:243], %12, 0\n\t"
      "v_cvt_pk_bf16_f32 v232, v208, v209\n\t"
      "v_cvt_pk_bf16_f32 v233, v210, v211\n\t"
      "v_exp_f32 v212, v212\n\t"
      "v_exp_f32 v213, v213\n\t"
      "v_exp_f32 v214, v214\n\t"
      "v_exp_f32 v215, v215\n\t"
      "v_mfma_f32_32x32x16_bf16 v[192:207], v[244:247], %13, v[192:207]\n\t"
      "v_cvt_pk_bf16_f32 v234, v212, v213\n\t"
      "v_cvt_pk_bf16_f32 v235, v214, v215\n\t"
      "v_exp_f32 v216, v216\n\t"
      "v_exp_f32 v217, v217\n\t"
      "v_exp_f32 v218, v218\n\t"
      "v_exp_f32 v219, v219\n\t"
      "v_mfma_f32_32x32x16_bf16 %0, v[184:187], v[224:227], %0\n\t"
      "v_cvt_pk_bf16_f32 v236, v216, v217\n\t"
      "v_cvt_pk_bf16_f32 v237, v218, v219\n\t"
      "v_exp_f32 v220, v220\n\t"
      "v_exp_f32 v221, v221\n\t"
      "v_exp_f32 v222, v222\n\t"
      "v_exp_f32 v223, v223\n\t"
      "v_mfma_f32_32x32x16_bf16 %0, v[188:191], v[228:231], %0\n\t"
      "v_cvt_pk_bf16_f32 v238, v220, v221\n\t"
      "v_cvt_pk_bf16_f32 v239, v222, v223\n\t"
      "v_exp_f32 v192, v192\n\t"
      "v_exp_f32 v193, v193\n\t"
      "v_exp_f32 v194, v194\n\t"
      "v_exp_f32 v195, v195\n\t"
      "v_mfma_f32_32x32x16_bf16 v[208:223], v[240:243], %14, 0\n\t"
      "ds_read_b128 v[240:243], %4 offset:24576\n\t"
      "ds_read_b64_tr_b16 v[248:249], %5 offset:24576\n\t"
      "v_cvt_pk_bf16_f32 v224, v192, v193\n\t"
      "v_cvt_pk_bf16_f32 v225, v194, v195\n\t"
      "v_exp_f32 v196, v196\n\t"
      "v_exp_f32 v197, v197\n\t"
      "v_exp_f32 v198, v198\n\t"
      "v_exp_f32 v199, v199\n\t"
      "v_mfma_f32_32x32x16_bf16 v[208:223], v[244:247], %15, v[208:223]\n\t"
      "ds_read_b128 v[244:247], %4 offset:25600\n\t"
      "ds_read_b64_tr_b16 v[250:251], %5 offset:25088\n\t"
      "v_cvt_pk_bf16_f32 v226, v196, v197\n\t"
      "v_cvt_pk_bf16_f32 v227, v198, v199\n\t"
      "v_exp_f32 v200, v200\n\t"
      "v_exp_f32 v201, v201\n\t"
      "v_exp_f32 v202, v202\n\t"
      "v_exp_f32 v203, v203\n\t"
      "v_mfma_f32_32x32x16_bf16 %1, v[184:187], v[232:235], %1\n\t"
      "ds_read_b64_tr_b16 v[252:253], %5 offset:25600\n\t"
      "v_cvt_pk_bf16_f32 v228, v200, v201\n\t"
      "v_cvt_pk_bf16_f32 v229, v202, v203\n\t"
      "v_exp_f32 v204, v204\n\t"
      "v_exp_f32 v205, v205\n\t"
      "v_exp_f32 v206, v206\n\t"
      "v_exp_f32 v207, v207\n\t"
      "v_mfma_f32_32x32x16_bf16 %1, v[188:191], v[236:239], %1\n\t"
      "ds_read_b64_tr_b16 v[254:255], %5 offset:26112\n\t"
      "v_cvt_pk_bf16_f32 v230, v204, v205\n\t"
      "v_cvt_pk_bf16_f32 v231, v206, v207\n\t"
      "v_exp_f32 v208, v208\n\t"
      "v_exp_f32 v209, v209\n\t"
      "v_exp_f32 v210, v210\n\t"
      "v_exp_f32 v211, v211\n\t"
      "s_waitcnt lgkmcnt(0)\n\t"
      "v_mfma_f32_32x32x16_bf16 v[192:207], v[240:243], %8, 0\n\t"
      "v_cvt_pk_bf16_f32 v232, v208, v209\n\t"
      "v_cvt_pk_bf16_f32 v233, v210, v211\n\t"
      "v_exp_f32 v212, v212\n\t"
      "v_exp_f32 v213, v213\n\t"
      "v_exp_f32 v214, v214\n\t"
      "v_exp_f32 v215, v215\n\t"
      "v_mfma_f32_32x32x16_bf16 v[192:207], v[244:247], %9, v[192:207]\n\t"
      "v_cvt_pk_bf16_f32 v234, v212, v213\n\t"
      "v_cvt_pk_bf16_f32 v235, v214, v215\n\t"
      "v_exp_f32 v216, v216\n\t"
      "v_exp_f32 v217, v217\n\t"
      "v_exp_f32 v218, v218\n\t"
      "v_exp_f32 v219, v219\n\t"
      "v_mfma_f32_32x32x16_bf16 %2, v[184:187], v[224:227], %2\n\t"
      "v_cvt_pk_bf16_f32 v236, v216, v217\n\t"
      "v_cvt_pk_bf16_f32 v237, v218, v219\n\t"
      "v_exp_f32 v220, v220\n\t"
      "v_exp_f32 v221, v221\n\t"
      "v_exp_f32 v222, v222\n\t"
      "v_exp_f32 v223, v223\n\t"
      "v_mfma_f32_32x32x16_bf16 %2, v[188:191], v[228:231], %2\n\t"
      "v_cvt_pk_bf16_f32 v238, v220, v221\n\t"
      "v_cvt_pk_bf16_f32 v239, v222, v223\n\t"
      "v_exp_f32 v192, v192\n\t"
      "v_exp_f32 v193, v193\n\t"
      "v_exp_f32 v194, v194\n\t"
      "v_exp_f32 v195, v195\n\t"
      "v_mfma_f32_32x32x16_bf16 v[208:223], v[240:243], %10, 0\n\t"
      "v_cvt_pk_bf16_f32 v224, v192, v193\n\t"
      "v_cvt_pk_bf16_f32 v225, v194, v195\n\t"
      "v_exp_f32 v196, v196\n\t"
      "v_exp_f32 v197, v197\n\t"
      "v_exp_f32 v198, v198\n\t"
      "v_exp_f32 v199, v199\n\t"
      "v_mfma_f32_32x32x16_bf16 v[208:223], v[244:247], %11, v[208:223]\n\t"
      "v_cvt_pk_bf16_f32 v226, v196, v197\n\t"
      "v_cvt_pk_bf16_f32 v227, v198, v199\n\t"
      "v_exp_f32 v200, v200\n\t"
      "v_exp_f32 v201, v201\n\t"
      "v_exp_f32 v202, v202\n\t"
      "v_exp_f32 v203, v203\n\t"
      "v_mfma_f32_32x32x16_bf16 %3, v[184:187], v[232:235], %3\n\t"
      "v_cvt_pk_bf16_f32 v228, v200, v201\n\t"
      "v_cvt_pk_bf16_f32 v229, v202, v203\n\t"
      "v_exp_f32 v204, v204\n\t"
      "v_exp_f32 v205, v205\n\t"
      "v_exp_f32 v206, v206\n\t"
      "v_exp_f32 v207, v207\n\t"
      "v_mfma_f32_32x32x16_bf16 %3, v[188:191], v[236:239], %3\n\t"
      "v_cvt_pk_bf16_f32 v230, v204, v205\n\t"
      "v_cvt_pk_bf16_f32 v231, v206, v207\n\t"
      "v_exp_f32 v208, v208\n\t"
      "v_exp_f32 v209, v209\n\t"
      "v_exp_f32 v210, v210\n\t"
      "v_exp_f32 v211, v211\n\t"
      "v_mfma_f32_32x32x16_bf16 v[192:207], v[240:243], %12, 0\n\t"
      "v_cvt_pk_bf16_f32 v232, v208, v209\n\t"
      "v_cvt_pk_bf16_f32 v233, v210, v211\n\t"
      "v_exp_f32 v212, v212\n\t"
      "v_exp_f32 v213, v213\n\t"
      "v_exp_f32 v214, v214\n\t"
      "v_exp_f32 v215, v215\n\t"
      "v_mfma_f32_32x32x16_bf16 v[192:207], v[244:247], %13, v[192:207]\n\t"
      "v_cvt_pk_bf16_f32 v234, v212, v213\n\t"
      "v_cvt_pk_bf16_f32 v235, v214, v215\n\t"
      "v_exp_f32 v216, v216\n\t"
      "v_exp_f32 v217, v217\n\t"
      "v_exp_f32 v218, v218\n\t"
      "v_exp_f32 v219, v219\n\t"
      "v_mfma_f32_32x32x16_bf16 %0, v[248:251], v[224:227], %0\n\t"
      "v_cvt_pk_bf16_f32 v236, v216, v217\n\t"
      "v_cvt_pk_bf16_f32 v237, v218, v219\n\t"
      "v_exp_f32 v220, v220\n\t"
      "v_exp_f32 v221, v221\n\t"
      "v_exp_f32 v222, v222\n\t"
      "v_exp_f32 v223, v223\n\t"
      "v_mfma_f32_32x32x16_bf16 %0, v[252:255], v[228:231], %0\n\t"
      "v_cvt_pk_bf16_f32 v238, v220, v221\n\t"
      "v_cvt_pk_bf16_f32 v239, v222, v223\n\t"
      "v_exp_f32 v192, v192\n\t"
      "v_exp_f32 v193, v193\n\t"
      "v_exp_f32 v194, v194\n\t"
      "v_exp_f32 v195, v195\n\t"
      "v_mfma_f32_32x32x16_bf16 v[208:223], v[240:243], %14, 0\n\t"
      "ds_read_b128 v[240:243], %4 offset:28672\n\t"
      "ds_read_b64_tr_b16 v[184:185], %5 offset:28672\n\t"
      "v_cvt_pk_bf16_f32 v224, v192, v193\n\t"
      "v_cvt_pk_bf16_f32 v225, v194, v195\n\t"
      "v_exp_f32 v196, v196\n\t"
      "v_exp_f32 v197, v197\n\t"
      "v_exp_f32 v198, v198\n\t"
      "v_exp_f32 v199, v199\n\t"
      "v_mfma_f32_32x32x16_bf16 v[208:223], v[244:247], %15, v[208:223]\n\t"
      "ds_read_b128 v[244:247], %4 offset:29696\n\t"
      "ds_read_b64_tr_b16 v[186:187], %5 offset:29184\n\t"
      "v_cvt_pk_bf16_f32 v226, v196, v197\n\t"
      "v_cvt_pk_bf16_f32 v227, v198, v199\n\t"
      "v_exp_f32 v200, v200\n\t"
      "v_exp_f32 v201, v201\n\t"
      "v_exp_f32 v202, v202\n\t"
      "v_exp_f32 v203, v203\n\t"
      "v_mfma_f32_32x32x16_bf16 %1, v[248:251], v[232:235], %1\n\t"
      "ds_read_b64_tr_b16 v[188:189], %5 offset:29696\n\t"
      "v_cvt_pk_bf16_f32 v228, v200, v201\n\t"
      "v_cvt_pk_bf16_f32 v229, v202, v203\n\t"
      "v_exp_f32 v204, v204\n\t"
      "v_exp_f32 v205, v205\n\t"
      "v_exp_f32 v206, v206\n\t"
      "v_exp_f32 v207, v207\n\t"
      "v_mfma_f32_32x32x16_bf16 %1, v[252:255], v[236:239], %1\n\t"
      "ds_read_b64_tr_b16 v[190:191], %5 offset:30208\n\t"
      "v_cvt_pk_bf16_f32 v230, v204, v205\n\t"
      "v_cvt_pk_bf16_f32 v231, v206, v207\n\t"
      "v_exp_f32 v208, v208\n\t"
      "v_exp_f32 v209, v209\n\t"
      "v_exp_f32 v210, v210\n\t"
      "v_exp_f32 v211, v211\n\t"
      "s_waitcnt lgkmcnt(0)\n\t"
      "v_mfma_f32_32x32x16_bf16 v[192:207], v[240:243], %8, 0\n\t"
      "v_cvt_pk_bf16_f32 v232, v208, v209\n\t"
      "v_cvt_pk_bf16_f32 v233, v210, v211\n\t"
      "v_exp_f32 v212, v212\n\t"
      "v_exp_f32 v213, v213\n\t"
      "v_exp_f32 v214, v214\n\t"
      "v_exp_f32 v215, v215\n\t"
      "v_mfma_f32_32x32x16_bf16 v[192:207], v[244:247], %9, v[192:207]\n\t"
      "v_cvt_pk_bf16_f32 v234, v212, v213\n\t"
      "v_cvt_pk_bf16_f32 v235, v214, v215\n\t"
      "v_exp_f32 v216, v216\n\t"
      "v_exp_f32 v217, v217\n\t"
      "v_exp_f32 v218, v218\n\t"
      "v_exp_f32 v219, v219\n\t"
      "v_mfma_f32_32x32x16_bf16 %2, v[248:251], v[224:227], %2\n\t"
      "v_cvt_pk_bf16_f32 v236, v216, v217\n\t"
      "v_cvt_pk_bf16_f32 v237, v218, v219\n\t"
      "v_exp_f32 v220, v220\n\t"
      "v_exp_f32 v221, v221\n\t"
      "v_exp_f32 v222, v222\n\t"
      "v_exp_f32 v223, v223\n\t"
      "v_mfma_f32_32x32x16_bf16 %2, v[252:255], v[228:231], %2\n\t"
      "v_cvt_pk_bf16_f32 v238, v220, v221\n\t"
      "v_cvt_pk_bf16_f32 v239, v222, v223\n\t"
      "v_exp_f32 v192, v192\n\t"
      "v_exp_f32 v193, v193\n\t"
      "v_exp_f32 v194, v194\n\t"
      "v_exp_f32 v195, v195\n\t"
      "v_mfma_f32_32x32x16_bf16 v[208:223], v[240:243], %10, 0\n\t"
      "v_cvt_pk_bf16_f32 v224, v192, v193\n\t"
      "v_cvt_pk_bf16_f32 v225, v194, v195\n\t"
      "v_exp_f32 v196, v196\n\t"
      "v_exp_f32 v197, v197\n\t"
      "v_exp_f32 v198, v198\n\t"
      "v_exp_f32 v199, v199\n\t"
      "v_mfma_f32_32x32x16_bf16 v[208:223], v[244:247], %11, v[208:223]\n\t"
      "v_cvt_pk_bf16_f32 v226, v196, v197\n\t"
      "v_cvt_pk_bf16_f32 v227, v198, v199\n\t"
      "v_exp_f32 v200, v200\n\t"
      "v_exp_f32 v201, v201\n\t"
      "v_exp_f32 v202, v202\n\t"
      "v_exp_f32 v203, v203\n\t"
      "v_mfma_f32_32x32x16_bf16 %3, v[248:251], v[232:235], %3\n\t"
      "v_cvt_pk_bf16_f32 v228, v200, v201\n\t"
      "v_cvt_pk_bf16_f32 v229, v202, v203\n\t"
      "v_exp_f32 v204, v204\n\t"
      "v_exp_f32 v205, v205\n\t"
      "v_exp_f32 v206, v206\n\t"
      "v_exp_f32 v207, v207\n\t"
      "v_mfma_f32_32x32x16_bf16 %3, v[252:255], v[236:239], %3\n\t"
      "v_cvt_pk_bf16_f32 v230, v204, v205\n\t"
      "v_cvt_pk_bf16_f32 v231, v206, v207\n\t"
      "v_exp_f32 v208, v208\n\t"
      "v_exp_f32 v209, v209\n\t"
      "v_exp_f32 v210, v210\n\t"
      "v_exp_f32 v211, v211\n\t"
      "v_mfma_f32_32x32x16_bf16 v[192:207], v[240:243], %12, 0\n\t"
      "v_cvt_pk_bf16_f32 v232, v208, v209\n\t"
      "v_cvt_pk_bf16_f32 v233, v210, v211\n\t"
      "v_exp_f32 v212, v212\n\t"
      "v_exp_f32 v213, v213\n\t"
      "v_exp_f32 v214, v214\n\t"
      "v_exp_f32 v215, v215\n\t"
      "v_mfma_f32_32x32x16_bf16 v[192:207], v[244:247], %13, v[192:207]\n\t"
      "v_cvt_pk_bf16_f32 v234, v212, v213\n\t"
      "v_cvt_pk_bf16_f32 v235, v214, v215\n\t"
      "v_exp_f32 v216, v216\n\t"
      "v_exp_f32 v217, v217\n\t"
      "v_exp_f32 v218, v218\n\t"
      "v_exp_f32 v219, v219\n\t"
      "v_mfma_f32_32x32x16_bf16 %0, v[184:187], v[224:227], %0\n\t"
      "v_cvt_pk_bf16_f32 v236, v216, v217\n\t"
      "v_cvt_pk_bf16_f32 v237, v218, v219\n\t"
      "v_exp_f32 v220, v220\n\t"
      "v_exp_f32 v221, v221\n\t"
      "v_exp_f32 v222, v222\n\t"
      "v_exp_f32 v223, v223\n\t"
      "v_mfma_f32_32x32x16_bf16 %0, v[188:191], v[228:231], %0\n\t"
      "v_cvt_pk_bf16_f32 v238, v220, v221\n\t"
      "v_cvt_pk_bf16_f32 v239, v222, v223\n\t"
      "v_add_u32 %4, 32768, %4\n\t"
      "v_add_u32 %5, 32768, %5\n\t"
      "s_sub_u32 %7, %7, 1\n\t"
      "s_cmp_lg_u32 %7, 0\n\t"
      "s_cbranch_scc1 LOUTER%=\n\t"
      "v_and_b32 %4, 0x1ffff, %4\n\t"
      "v_and_b32 %5, 0x1ffff, %5\n\t"
      "s_mov_b64 vcc, -1\n\t"
      "s_branch LTOP%=\n\t"
      "LDONE%=:\n\t"
      "s_nop 1\n\t"
      "v_mfma_f32_32x32x16_bf16 %3, v[184:187], v[232:235], %3\n\t"
      "v_mfma_f32_32x32x16_bf16 %3, v[188:191], v[236:239], %3\n\t"
      "s_nop 11\n\t"
      : "+v"(o[0]), "+v"(o[1]), "+v"(o[2]), "+v"(o[3]), "+v"(kl), "+v"(vl), "=&s"(cnt), "=&s"(cch)
      : "v"(qf[0][0]), "v"(qf[0][1]), "v"(qf[1][0]), "v"(qf[1][1]), "v"(qf[2][0]), "v"(qf[2][1]), "v"(qf[3][0]), "v"(qf[3][1])
      : "memory", "scc", "vcc", "v184", "v185", "v186", "v187", "v188", "v189", "v190", "v191", "v192", "v193", "v194", "v195", "v196", "v197", "v198", "v199", "v200", "v201", "v202", "v203", "v204", "v205", "v206", "v207", "v208", "v209", "v210", "v211", "v212", "v213", "v214", "v215", "v216", "v217", "v218", "v219", "v220", "v221", "v222", "v223", "v224", "v225", "v226", "v227", "v228", "v229", "v230", "v231", "v232", "v233", "v234", "v235", "v236", "v237", "v238", "v239", "v240", "v241", "v242", "v243", "v244", "v245", "v246", "v247", "v248", "v249", "v250", "v251", "v252", "v253", "v254", "v255");
  } else {
    float m[4] = {-INFINITY, -INFINITY, -INFINITY, -INFINITY};
#pragma unroll 1
    for (int tl = 0; tl < SEQ / 32; ++tl) {
      if ((tl & (TPC - 1)) == 0) asm volatile("s_waitcnt lgkmcnt(0)\n\ts_barrier" ::: "memory");
      const unsigned boff = (tl & (NSTAGE * TPC - 1)) * BLK;
      LDSP(const char) kp = (LDSP(const char))(size_t)(kl + boff);
      LDSP(const char) vp = (LDSP(const char))(size_t)(vl + boff);
      const f16x8 k0 = kfrag(kp), k1 = kfrag(kp + 1024);
      const bf16x8 v0 = vfrag(vp), v1 = vfrag(vp + 1024);
#pragma unroll
      for (int b = 0; b < 4; ++b) {
        f32x16 s = mfma_qk(k0, __builtin_bit_cast(f16x8, qf[b][0]), (f32x16){});
        s = mfma_qk(k1, __builtin_bit_cast(f16x8, qf[b][1]), s);
        float mx = m[b];
#pragma unroll
        for (int i = 0; i < 16; ++i) { s[i] *= c; mx = fmaxf(mx, s[i]); }
        mx = swap_max(mx);
        const float al = __builtin_amdgcn_exp2f(m[b] - mx);
        m[b] = mx;
#pragma unroll
        for (int i = 0; i < 16; ++i) { s[i] = __builtin_amdgcn_exp2f(s[i] - mx); o[b][i] *= al; }
        const u32x4 pa = {pk_bf16(s[0], s[1]), pk_bf16(s[2], s[3]), pk_bf16(s[4], s[5]), pk_bf16(s[6], s[7])};
        const u32x4 pb = {pk_bf16(s[8], s[9]), pk_bf16(s[10], s[11]), pk_bf16(s[12], s[13]), pk_bf16(s[14], s[15])};
        o[b] = mfma_pv(v0, __builtin_bit_cast(bf16x8, pa), o[b]);
        o[b] = mfma_pv(v1, __builtin_bit_cast(bf16x8, pb), o[b]);
      }
    }
  }
  STAMP(2);
  char* Ow = out + hoff + (size_t)(q0 + wid * 128 + r) * ROWB + h * 8;
#pragma unroll
  for (int b = 0; b < 4; ++b) {
    auto rr = __builtin_amdgcn_permlane32_swap(__float_as_uint(o[b][12]), __float_as_uint(o[b][12]), false, false);
    const float rl = __builtin_amdgcn_rcpf(__uint_as_float(rr[1]));
    char* op = Ow + b * 32 * ROWB;
#pragma unroll
    for (int g = 0; g < 4; ++g) {
      const u32x2 w = {pk_bf16(o[b][4 * g] * rl, o[b][4 * g + 1] * rl), pk_bf16(o[b][4 * g + 2] * rl, o[b][4 * g + 3] * rl)};
      if (g < 3 || h == 0) *(u32x2*)(op + g * 16) = w;
    }
  }
  STAMP(3);
  STAMP_OUT;
}

extern "C" void kernel_launch(void* const* d_in, const int* in_sizes, int n_in, void* d_out, int out_size, void* d_ws, size_t ws_size,
                              hipStream_t stream) {
  (void)hipFuncSetAttribute((const void*)qkn_attn, hipFuncAttributeMaxDynamicSharedMemorySize, LDS_BYTES);
  qkn_attn<<<256, 512, LDS_BYTES, stream>>>((const char*)d_in[0], (const char*)d_in[1], (const char*)d_in[2],
                                            (const unsigned*)d_in[3], (char*)d_out, (unsigned long long*)d_ws);
}
```

```cpp
#include <hip/hip_runtime.h>
#include <hip/hip_bf16.h>

typedef _Float16 f16x8 __attribute__((ext_vector_type(8)));
typedef _Float16 f16x2 __attribute__((ext_vector_type(2)));
typedef __bf16 bf16x8 __attribute__((ext_vector_type(8)));
typedef __bf16 bf16x2 __attribute__((ext_vector_type(2)));
typedef short s16x4 __attribute__((ext_vector_type(4)));
typedef float f32x16 __attribute__((ext_vector_type(16)));
typedef float f32x2 __attribute__((ext_vector_type(2)));
typedef unsigned u32x4 __attribute__((ext_vector_type(4)));
typedef unsigned u32x2 __attribute__((ext_vector_type(2)));
typedef unsigned u32x4a8 __attribute__((ext_vector_type(4), aligned(8)));

#define DI __device__ __forceinline__
#define LDSP(T) __attribute__((address_space(3))) T*

constexpr int SEQ = 2048, DH = 28, NHEAD = 16, ROWB = DH * 2;
constexpr int CHUNK = 256, NCHUNK = SEQ / CHUNK, TPC = CHUNK / 32;
constexpr int BLK = 4096, STAGE = TPC * BLK, NSTAGE = 4, LDS_BYTES = NSTAGE * STAGE;
constexpr float LOG2E = 1.4426950408889634f;
constexpr float FAST_LIMIT = 24.0f;
constexpr float EPS = 1e-6f;

DI unsigned pk_bf16(float lo, float hi) { f32x2 v = {lo, hi}; return __builtin_bit_cast(unsigned, __builtin_convertvector(v, bf16x2)); }
DI unsigned pk_f16(float lo, float hi) { f32x2 v = {lo, hi}; return __builtin_bit_cast(unsigned, __builtin_convertvector(v, f16x2)); }
DI float bf_lo(unsigned w) { return __uint_as_float(w << 16); }
DI float bf_hi(unsigned w) { return __uint_as_float(w & 0xffff0000u); }
DI float dot2(unsigned a, float c) {
  return __builtin_amdgcn_fdot2_f32_bf16(__builtin_bit_cast(bf16x2, a), __builtin_bit_cast(bf16x2, a), c, false);
}
DI unsigned scale_pk(unsigned w, float r) { return pk_bf16(bf_lo(w) * r, bf_hi(w) * r); }
DI float swap_sum(float x) {
  auto rr = __builtin_amdgcn_permlane32_swap(__float_as_uint(x), __float_as_uint(x), false, false);
  return __uint_as_float(rr[0]) + __uint_as_float(rr[1]);
}
DI float swap_max(float x) {
  auto rr = __builtin_amdgcn_permlane32_swap(__float_as_uint(x), __float_as_uint(x), false, false);
  return fmaxf(__uint_as_float(rr[0]), __uint_as_float(rr[1]));
}
DI f32x16 mfma_qk(f16x8 a, f16x8 b, f32x16 c) { return __builtin_amdgcn_mfma_f32_32x32x16_f16(a, b, c, 0, 0, 0); }
DI f32x16 mfma_pv(bf16x8 a, bf16x8 b, f32x16 c) { return __builtin_amdgcn_mfma_f32_32x32x16_bf16(a, b, c, 0, 0, 0); }

struct KRaw { u32x4 a, b, c; u32x2 d; };

DI KRaw k_load(const char* p) {
  KRaw r;
  r.a = *(const u32x4a8*)(p); r.b = *(const u32x4a8*)(p + 16); r.c = *(const u32x4a8*)(p + 32); r.d = *(const u32x2*)(p + 48);
  return r;
}
DI void k_norm_store(const KRaw& k, LDSP(char) dst, unsigned pad0) {
  float s0 = dot2(k.a[0], 0.f), s1 = dot2(k.a[1], 0.f);
  s0 = dot2(k.a[2], s0); s1 = dot2(k.a[3], s1);
  s0 = dot2(k.b[0], s0); s1 = dot2(k.b[1], s1); s0 = dot2(k.b[2], s0); s1 = dot2(k.b[3], s1);
  s0 = dot2(k.c[0], s0); s1 = dot2(k.c[1], s1); s0 = dot2(k.c[2], s0); s1 = dot2(k.c[3], s1);
  s0 = dot2(k.d[0], s0); s1 = dot2(k.d[1], s1);
  const float r = __builtin_amdgcn_rcpf(__builtin_amdgcn_sqrtf(s0 + s1) + EPS);
  u32x4 o;
  o = (u32x4){scale_pk(k.a[0], r), scale_pk(k.a[1], r), scale_pk(k.a[2], r), scale_pk(k.a[3], r)};
  *(LDSP(u32x4))(dst) = o;
  o = (u32x4){scale_pk(k.b[0], r), scale_pk(k.b[1], r), scale_pk(k.b[2], r), scale_pk(k.b[3], r)};
  *(LDSP(u32x4))(dst + 512) = o;
  o = (u32x4){scale_pk(k.c[0], r), scale_pk(k.c[1], r), scale_pk(k.c[2], r), scale_pk(k.c[3], r)};
  *(LDSP(u32x4))(dst + 1024) = o;
  o = (u32x4){scale_pk(k.d[0], r), scale_pk(k.d[1], r), pad0, 0u};
  *(LDSP(u32x4))(dst + 1536) = o;
}

DI void v_dma(const char* gbase  , unsigned voff  , unsigned ldsdst  ) {
  unsigned keep; unsigned long long ex;
  asm volatile(
      "s_mov_b32 %0, m0\n\t"
      "s_mov_b64 %1, exec\n\t"
      "s_mov_b32 exec_lo, 0x3fff3fff\n\t"
      "s_mov_b32 exec_hi, 0x3fff3fff\n\t"
      "s_mov_b32 m0, %4\n\t"
      "s_nop 0\n\t"
      "global_load_lds_dword %2, %3 offset:0\n\t"    "s_add_u32 m0, m0, 32\n\t" "s_nop 0\n\t"
      "global_load_lds_dword %2, %3 offset:224\n\t"  "s_add_u32 m0, m0, 32\n\t" "s_nop 0\n\t"
      "global_load_lds_dword %2, %3 offset:448\n\t"  "s_add_u32 m0, m0, 32\n\t" "s_nop 0\n\t"
      "global_load_lds_dword %2, %3 offset:672\n\t"  "s_add_u32 m0, m0, 32\n\t" "s_nop 0\n\t"
      "global_load_lds_dword %2, %3 offset:896\n\t"  "s_add_u32 m0, m0, 32\n\t" "s_nop 0\n\t"
      "global_load_lds_dword %2, %3 offset:1120\n\t" "s_add_u32 m0, m0, 32\n\t" "s_nop 0\n\t"
      "global_load_lds_dword %2, %3 offset:1344\n\t" "s_add_u32 m0, m0, 32\n\t" "s_nop 0\n\t"
      "global_load_lds_dword %2, %3 offset:1568\n\t" "s_add_u32 m0, m0, 2080\n\t" "s_nop 0\n\t"
      "global_load_lds_dword %2, %3 offset:1792\n\t" "s_add_u32 m0, m0, 32\n\t" "s_nop 0\n\t"
      "global_load_lds_dword %2, %3 offset:2016\n\t" "s_add_u32 m0, m0, 32\n\t" "s_nop 0\n\t"
      "global_load_lds_dword %2, %3 offset:2240\n\t" "s_add_u32 m0, m0, 32\n\t" "s_nop 0\n\t"
      "global_load_lds_dword %2, %3 offset:2464\n\t" "s_add_u32 m0, m0, 32\n\t" "s_nop 0\n\t"
      "global_load_lds_dword %2, %3 offset:2688\n\t" "s_add_u32 m0, m0, 32\n\t" "s_nop 0\n\t"
      "global_load_lds_dword %2, %3 offset:2912\n\t" "s_add_u32 m0, m0, 32\n\t" "s_nop 0\n\t"
      "global_load_lds_dword %2, %3 offset:3136\n\t" "s_add_u32 m0, m0, 32\n\t" "s_nop 0\n\t"
      "global_load_lds_dword %2, %3 offset:3360\n\t"
      "s_mov_b64 exec, %1\n\t"
      "s_mov_b32 m0, %0"
      : "=&s"(keep), "=&s"(ex)
      : "v"(voff), "s"(gbase), "s"(ldsdst)
      : "memory", "scc");
}

DI s16x4 vtr(LDSP(const char) p) {
  typedef short v4s __attribute__((ext_vector_type(4)));
  return __builtin_amdgcn_ds_read_tr16_b64_v4i16((LDSP(v4s))p);
}
DI bf16x8 vfrag(LDSP(const char) p) {
  s16x4 lo = vtr(p), hi = vtr(p + 512);
  return __builtin_bit_cast(bf16x8, __builtin_shufflevector(lo, hi, 0, 1, 2, 3, 4, 5, 6, 7));
}
DI f16x8 kfrag(LDSP(const char) p) { return *(LDSP(const f16x8))p; }

#define STAMP(i)
#define STAMP_DECL
#define STAMP_OUT

__global__ __launch_bounds__(512, 2) void qkn_attn(const char* __restrict__ q, const char* __restrict__ k, const char* __restrict__ v,
                                                   const unsigned* __restrict__ t, char* __restrict__ out, unsigned long long* ws) {
  extern __shared__ __attribute__((aligned(16))) char smem[];
  STAMP_DECL;
  STAMP(0);
  const int bid = blockIdx.x, x = bid & 7, y = bid >> 3;
  const int head = x * 8 + (y >> 2), q0 = (y & 3) * 512;
  const size_t hoff = (size_t)head * SEQ * ROWB;
  LDSP(char) lds = (LDSP(char))smem;
  const unsigned lds0 = (unsigned)(size_t)lds;
  const int tid = threadIdx.x, lane = tid & 63, r = lane & 31, h = lane >> 5;
  const int wid = __builtin_amdgcn_readfirstlane(tid >> 6);
  const unsigned tw = t[(head & (NHEAD - 1)) >> 1];
  const float c = __uint_as_float((head & 1) ? (tw & 0xffff0000u) : (tw << 16)) * LOG2E;
  const bool fast = fabsf(c) <= FAST_LIMIT;

  if (wid >= 4) {
    const int pw = wid - 4, ptid = tid - 256;
    const char* Kh = k + hoff + (size_t)ptid * ROWB;
    const char* Vh = v + hoff + (size_t)pw * 64 * ROWB;
    const unsigned voff = (unsigned)((lane >> 4) * ROWB + (lane & 15) * 4);
    const unsigned vdst = lds0 + pw * 2 * BLK + 2048;
    LDSP(char) kdst = lds + (ptid >> 5) * BLK + (ptid & 31) * 16;
    KRaw ka = k_load(Kh);
    KRaw kb = k_load(Kh + CHUNK * ROWB);
    v_dma(Vh, voff, vdst);
    {
      const u32x2 one = {0x00003F80u, 0u};
#pragma unroll
      for (int s = 0; s < NSTAGE; ++s) *(LDSP(u32x2))(lds + s * STAGE + (ptid >> 5) * BLK + 2048 + (ptid & 31) * 64 + 56) = one;
    }
    const unsigned kpad = fast ? pk_bf16(-fabsf(c), 0.f) : 0u;
    k_norm_store(ka, kdst, kpad);
    v_dma(Vh + CHUNK * ROWB, voff, vdst + STAGE);
    asm volatile("s_waitcnt vmcnt(16) lgkmcnt(0)\n\ts_barrier" ::: "memory");
#pragma unroll 1
    for (int ch = 1; ch < NCHUNK; ++ch) {
      __builtin_amdgcn_s_waitcnt(0x0F70);
      ka = kb;
      if (ch + 1 < NCHUNK) {
        kb = k_load(Kh + (size_t)(ch + 1) * CHUNK * ROWB);
        v_dma(Vh + (size_t)(ch + 1) * CHUNK * ROWB, voff, vdst + ((ch + 1) & (NSTAGE - 1)) * STAGE);
      }
      k_norm_store(ka, kdst + (ch & (NSTAGE - 1)) * STAGE, kpad);
      asm volatile("s_waitcnt lgkmcnt(0)\n\ts_barrier" ::: "memory");
    }
    return;
  }

  const char* Qw = q + hoff + (size_t)(q0 + wid * 128 + r) * ROWB + h * 16;
  u32x4 qa[4]; u32x2 qb[4], qc[4];
#pragma unroll
  for (int b = 0; b < 4; ++b) {
    const char* qp = Qw + b * 32 * ROWB;
    qa[b] = *(const u32x4a8*)qp;
    qb[b] = *(const u32x2*)(qp + 32);
    qc[b] = *(const u32x2*)(qp + (h ? 32 : 40));
  }
  u32x4 qf[4][2];
  const unsigned hmask = h ? 0u : 0xffffffffu;
  const float qscale = fast ? c : 1.0f;
  const unsigned qpad = h ? 0x00003F80u : 0u;
#pragma unroll
  for (int b = 0; b < 4; ++b) {
    qc[b][0] &= hmask; qc[b][1] &= hmask;
    float s0 = dot2(qa[b][0], 0.f), s1 = dot2(qa[b][1], 0.f);
    s0 = dot2(qa[b][2], s0); s1 = dot2(qa[b][3], s1);
    s0 = dot2(qb[b][0], s0); s1 = dot2(qb[b][1], s1);
    s0 = dot2(qc[b][0], s0); s1 = dot2(qc[b][1], s1);
    const float ss = swap_sum(s0 + s1);
    const float rq = qscale * __builtin_amdgcn_rcpf(__builtin_amdgcn_sqrtf(ss) + EPS);
    qf[b][0] = (u32x4){scale_pk(qa[b][0], rq), scale_pk(qa[b][1], rq), scale_pk(qa[b][2], rq), scale_pk(qa[b][3], rq)};
    qf[b][1] = (u32x4){scale_pk(qb[b][0], rq), scale_pk(qb[b][1], rq), scale_pk(qc[b][0], rq) | qpad, scale_pk(qc[b][1], rq)};
  }
  f32x16 o[4] = {};
  unsigned kl = lds0 + h * 512 + r * 16;
  unsigned vl = lds0 + 2048 + (4 * h + ((lane & 15) >> 2)) * 64 + ((lane >> 4) & 1) * 32 + (lane & 3) * 8;
  STAMP(1);
  if (fast) {
    unsigned cnt, cch;
    asm volatile(
      "s_setprio 3\n\t"
      "v_mov_b32 v232, 0\n\t"
      "v_mov_b32 v233, 0\n\t"
      "v_mov_b32 v234, 0\n\t"
      "v_mov_b32 v235, 0\n\t"
      "v_mov_b32 v236, 0\n\t"
      "v_mov_b32 v237, 0\n\t"
      "v_mov_b32 v238, 0\n\t"
      "v_mov_b32 v239, 0\n\t"
      "v_mov_b32 v184, 0\n\t"
      "v_mov_b32 v185, 0\n\t"
      "v_mov_b32 v186, 0\n\t"
      "v_mov_b32 v187, 0\n\t"
      "v_mov_b32 v188, 0\n\t"
      "v_mov_b32 v189, 0\n\t"
      "v_mov_b32 v190, 0\n\t"
      "v_mov_b32 v191, 0\n\t"
      "s_mov_b64 vcc, 0\n\t"
      "s_barrier\n\t"
      "ds_read_b128 v[240:243], %4 offset:0\n\t"
      "ds_read_b128 v[244:247], %4 offset:1024\n\t"
      "ds_read_b64_tr_b16 v[248:249], %5 offset:0\n\t"
      "ds_read_b64_tr_b16 v[250:251], %5 offset:512\n\t"
      "ds_read_b64_tr_b16 v[252:253], %5 offset:1024\n\t"
      "ds_read_b64_tr_b16 v[254:255], %5 offset:1536\n\t"
      "s_mov_b32 %6, 4\n\t"
      "s_mov_b32 %7, 8\n\t"
      "s_waitcnt lgkmcnt(4)\n\t"
      "v_mfma_f32_32x32x16_bf16 v[192:207], v[240:243], %8, 0\n\t"
      "v_mfma_f32_32x32x16_bf16 v[192:207], v[244:247], %9, v[192:207]\n\t"
      "s_waitcnt lgkmcnt(0)\n\t"
      "s_nop 10\n\t"
      "s_branch LMID%=\n\t"
      "LOUTER%=:\n\t"
      "s_barrier\n\t"
      "v_and_b32 %4, 0x1ffff, %4\n\t"
      "v_and_b32 %5, 0x1ffff, %5\n\t"
      "s_mov_b32 %6, 4\n\t"
      "LTOP%=:\n\t"
      "v_exp_f32 v192, v192\n\t"
      "v_exp_f32 v193, v193\n\t"
      "v_exp_f32 v194, v194\n\t"
      "v_exp_f32 v195, v195\n\t"
      "v_mfma_f32_32x32x16_bf16 v[208:223], v[240:243], %14, 0\n\t"
      "ds_read_b128 v[240:243], %4 offset:0\n\t"
      "ds_read_b64_tr_b16 v[248:249], %5 offset:0\n\t"
      "v_cvt_pk_bf16_f32 v224, v192, v193\n\t"
      "v_cvt_pk_bf16_f32 v225, v194, v195\n\t"
      "v_exp_f32 v196, v196\n\t"
      "v_exp_f32 v197, v197\n\t"
      "v_exp_f32 v198, v198\n\t"
      "v_exp_f32 v199, v199\n\t"
      "v_mfma_f32_32x32x16_bf16 v[208:223], v[244:247], %15, v[208:223]\n\t"
      "ds_read_b128 v[244:247], %4 offset:1024\n\t"
      "ds_read_b64_tr_b16 v[250:251], %5 offset:512\n\t"
      "v_cvt_pk_bf16_f32 v226, v196, v197\n\t"
      "v_cvt_pk_bf16_f32 v227, v198, v199\n\t"
      "v_exp_f32 v200, v200\n\t"
      "v_exp_f32 v201, v201\n\t"
      "v_exp_f32 v202, v202\n\t"
      "v_exp_f32 v203, v203\n\t"
      "v_mfma_f32_32x32x16_bf16 %1, v[184:187], v[232:235], %1\n\t"
      "ds_read_b64_tr_b16 v[252:253], %5 offset:1024\n\t"
      "v_cvt_pk_bf16_f32 v228, v200, v201\n\t"
      "v_cvt_pk_bf16_f32 v229, v202, v203\n\t"
      "v_exp_f32 v204, v204\n\t"
      "v_exp_f32 v205, v205\n\t"
      "v_exp_f32 v206, v206\n\t"
      "v_exp_f32 v207, v207\n\t"
      "v_mfma_f32_32x32x16_bf16 %1, v[188:191], v[236:239], %1\n\t"
      "ds_read_b64_tr_b16 v[254:255], %5 offset:1536\n\t"
      "v_cvt_pk_bf16_f32 v230, v204, v205\n\t"
      "v_cvt_pk_bf16_f32 v231, v206, v207\n\t"
      "v_exp_f32 v208, v208\n\t"
      "v_exp_f32 v209, v209\n\t"
      "v_exp_f32 v210, v210\n\t"
      "v_exp_f32 v211, v211\n\t"
      "s_waitcnt lgkmcnt(0)\n\t"
      "v_mfma_f32_32x32x16_bf16 v[192:207], v[240:243], %8, 0\n\t"
      "v_cvt_pk_bf16_f32 v232, v208, v209\n\t"
      "v_cvt_pk_bf16_f32 v233, v210, v211\n\t"
      "v_exp_f32 v212, v212\n\t"
      "v_exp_f32 v213, v213\n\t"
      "v_exp_f32 v214, v214\n\t"
      "v_exp_f32 v215, v215\n\t"
      "v_mfma_f32_32x32x16_bf16 v[192:207], v[244:247], %9, v[192:207]\n\t"
      "v_cvt_pk_bf16_f32 v234, v212, v213\n\t"
      "v_cvt_pk_bf16_f32 v235, v214, v215\n\t"
      "v_exp_f32 v216, v216\n\t"
      "v_exp_f32 v217, v217\n\t"
      "v_exp_f32 v218, v218\n\t"
      "v_exp_f32 v219, v219\n\t"
      "v_mfma_f32_32x32x16_bf16 %2, v[184:187], v[224:227], %2\n\t"
      "v_cvt_pk_bf16_f32 v236, v216, v217\n\t"
      "v_cvt_pk_bf16_f32 v237, v218, v219\n\t"
      "v_exp_f32 v220, v220\n\t"
      "v_exp_f32 v221, v221\n\t"
      "v_exp_f32 v222, v222\n\t"
      "v_exp_f32 v223, v223\n\t"
      "v_mfma_f32_32x32x16_bf16 %2, v[188:191], v[228:231], %2\n\t"
      "v_cvt_pk_bf16_f32 v238, v220, v221\n\t"
      "v_cvt_pk_bf16_f32 v239, v222, v223\n\t"
      "s_cbranch_vccnz LDONE%=\n\t"
      "LMID%=:\n\t"
      "v_exp_f32 v192, v192\n\t"
      "v_exp_f32 v193, v193\n\t"
      "v_exp_f32 v194, v194\n\t"
      "v_exp_f32 v195, v195\n\t"
      "v_mfma_f32_32x32x16_bf16 v[208:223], v[240:243], %10, 0\n\t"
      "v_cvt_pk_bf16_f32 v224, v192, v193\n\t"
      "v_cvt_pk_bf16_f32 v225, v194, v195\n\t"
      "v_exp_f32 v196, v196\n\t"
      "v_exp_f32 v197, v197\n\t"
      "v_exp_f32 v198, v198\n\t"
      "v_exp_f32 v199, v199\n\t"
      "v_mfma_f32_32x32x16_bf16 v[208:223], v[244:247], %11, v[208:223]\n\t"
      "v_cvt_pk_bf16_f32 v226, v196, v197\n\t"
      "v_cvt_pk_bf16_f32 v227, v198, v199\n\t"
      "v_exp_f32 v200, v200\n\t"
      "v_exp_f32 v201, v201\n\t"
      "v_exp_f32 v202, v202\n\t"
      "v_exp_f32 v203, v203\n\t"
      "v_mfma_f32_32x32x16_bf16 %3, v[184:187], v[232:235], %3\n\t"
      "v_cvt_pk_bf16_f32 v228, v200, v201\n\t"
      "v_cvt_pk_bf16_f32 v229, v202, v203\n\t"
      "v_exp_f32 v204, v204\n\t"
      "v_exp_f32 v205, v205\n\t"
      "v_exp_f32 v206, v206\n\t"
      "v_exp_f32 v207, v207\n\t"
      "v_mfma_f32_32x32x16_bf16 %3, v[188:191], v[236:239], %3\n\t"
      "v_cvt_pk_bf16_f32 v230, v204, v205\n\t"
      "v_cvt_pk_bf16_f32 v231, v206, v207\n\t"
      "v_exp_f32 v208, v208\n\t"
      "v_exp_f32 v209, v209\n\t"
      "v_exp_f32 v210, v210\n\t"
      "v_exp_f32 v211, v211\n\t"
      "v_mfma_f32_32x32x16_bf16 v[192:207], v[240:243], %12, 0\n\t"
      "v_cvt_pk_bf16_f32 v232, v208, v209\n\t"
      "v_cvt_pk_bf16_f32 v233, v210, v211\n\t"
      "v_exp_f32 v212, v212\n\t"
      "v_exp_f32 v213, v213\n\t"
      "v_exp_f32 v214, v214\n\t"
      "v_exp_f32 v215, v215\n\t"
      "v_mfma_f32_32x32x16_bf16 v[192:207], v[244:247], %13, v[192:207]\n\t"
      "v_cvt_pk_bf16_f32 v234, v212, v213\n\t"
      "v_cvt_pk_bf16_f32 v235, v214, v215\n\t"
      "v_exp_f32 v216, v216\n\t"
      "v_exp_f32 v217, v217\n\t"
      "v_exp_f32 v218, v218\n\t"
      "v_exp_f32 v219, v219\n\t"
      "v_mfma_f32_32x32x16_bf16 %0, v[248:251], v[224:227], %0\n\t"
      "v_cvt_pk_bf16_f32 v236, v216, v217\n\t"
      "v_cvt_pk_bf16_f32 v237, v218, v219\n\t"
      "v_exp_f32 v220, v220\n\t"
      "v_exp_f32 v221, v221\n\t"
      "v_exp_f32 v222, v222\n\t"
      "v_exp_f32 v223, v223\n\t"
      "v_mfma_f32_32x32x16_bf16 %0, v[252:255], v[228:231], %0\n\t"
      "v_cvt_pk_bf16_f32 v238, v220, v221\n\t"
      "v_cvt_pk_bf16_f32 v239, v222, v223\n\t"
      "v_exp_f32 v192, v192\n\t"
      "v_exp_f32 v193, v193\n\t"
      "v_exp_f32 v194, v194\n\t"
      "v_exp_f32 v195, v195\n\t"
      "v_mfma_f32_32x32x16_bf16 v[208:223], v[240:243], %14, 0\n\t"
      "ds_read_b128 v[240:243], %4 offset:4096\n\t"
      "ds_read_b64_tr_b16 v[184:185], %5 offset:4096\n\t"
      "v_cvt_pk_bf16_f32 v224, v192, v193\n\t"
      "v_cvt_pk_bf16_f32 v225, v194, v195\n\t"
      "v_exp_f32 v196, v196\n\t"
      "v_exp_f32 v197, v197\n\t"
      "v_exp_f32 v198, v198\n\t"
      "v_exp_f32 v199, v199\n\t"
      "v_mfma_f32_32x32x16_bf16 v[208:223], v[244:247], %15, v[208:223]\n\t"
      "ds_read_b128 v[244:247], %4 offset:5120\n\t"
      "ds_read_b64_tr_b16 v[186:187], %5 offset:4608\n\t"
      "v_cvt_pk_bf16_f32 v226, v196, v197\n\t"
      "v_cvt_pk_bf16_f32 v227, v198, v199\n\t"
      "v_exp_f32 v200, v200\n\t"
      "v_exp_f32 v201, v201\n\t"
      "v_exp_f32 v202, v202\n\t"
      "v_exp_f32 v203, v203\n\t"
      "v_mfma_f32_32x32x16_bf16 %1, v[248:251], v[232:235], %1\n\t"
      "ds_read_b64_tr_b16 v[188:189], %5 offset:5120\n\t"
      "v_cvt_pk_bf16_f32 v228, v200, v201\n\t"
      "v_cvt_pk_bf16_f32 v229, v202, v203\n\t"
      "v_exp_f32 v204, v204\n\t"
      "v_exp_f32 v205, v205\n\t"
      "v_exp_f32 v206, v206\n\t"
      "v_exp_f32 v207, v207\n\t"
      "v_mfma_f32_32x32x16_bf16 %1, v[252:255], v[236:239], %1\n\t"
      "ds_read_b64_tr_b16 v[190:191], %5 offset:5632\n\t"
      "v_cvt_pk_bf16_f32 v230, v204, v205\n\t"
      "v_cvt_pk_bf16_f32 v231, v206, v207\n\t"
      "v_exp_f32 v208, v208\n\t"
      "v_exp_f32 v209, v209\n\t"
      "v_exp_f32 v210, v210\n\t"
      "v_exp_f32 v211, v211\n\t"
      "s_waitcnt lgkmcnt(0)\n\t"
      "v_mfma_f32_32x32x16_bf16 v[192:207], v[240:243], %8, 0\n\t"
      "v_cvt_pk_bf16_f32 v232, v208, v209\n\t"
      "v_cvt_pk_bf16_f32 v233, v210, v211\n\t"
      "v_exp_f32 v212, v212\n\t"
      "v_exp_f32 v213, v213\n\t"
      "v_exp_f32 v214, v214\n\t"
      "v_exp_f32 v215, v215\n\t"
      "v_mfma_f32_32x32x16_bf16 v[192:207], v[244:247], %9, v[192:207]\n\t"
      "v_cvt_pk_bf16_f32 v234, v212, v213\n\t"
      "v_cvt_pk_bf16_f32 v235, v214, v215\n\t"
      "v_exp_f32 v216, v216\n\t"
      "v_exp_f32 v217, v217\n\t"
      "v_exp_f32 v218, v218\n\t"
      "v_exp_f32 v219, v219\n\t"
      "v_mfma_f32_32x32x16_bf16 %2, v[248:251], v[224:227], %2\n\t"
      "v_cvt_pk_bf16_f32 v236, v216, v217\n\t"
      "v_cvt_pk_bf16_f32 v237, v218, v219\n\t"
      "v_exp_f32 v220, v220\n\t"
      "v_exp_f32 v221, v221\n\t"
      "v_exp_f32 v222, v222\n\t"
      "v_exp_f32 v223, v223\n\t"
      "v_mfma_f32_32x32x16_bf16 %2, v[252:255], v[228:231], %2\n\t"
      "v_cvt_pk_bf16_f32 v238, v220, v221\n\t"
      "v_cvt_pk_bf16_f32 v239, v222, v223\n\t"
      "v_exp_f32 v192, v192\n\t"
      "v_exp_f32 v193, v193\n\t"
      "v_exp_f32 v194, v194\n\t"
      "v_exp_f32 v195, v195\n\t"
      "v_mfma_f32_32x32x16_bf16 v[208:223], v[240:243], %10, 0\n\t"
      "v_cvt_pk_bf16_f32 v224, v192, v193\n\t"
      "v_cvt_pk_bf16_f32 v225, v194, v195\n\t"
      "v_exp_f32 v196, v196\n\t"
      "v_exp_f32 v197, v197\n\t"
      "v_exp_f32 v198, v198\n\t"
      "v_exp_f32 v199, v199\n\t"
      "v_mfma_f32_32x32x16_bf16 v[208:223], v[244:247], %11, v[208:223]\n\t"
      "v_cvt_pk_bf16_f32 v226, v196, v197\n\t"
      "v_cvt_pk_bf16_f32 v227, v198, v199\n\t"
      "v_exp_f32 v200, v200\n\t"
      "v_exp_f32 v201, v201\n\t"
      "v_exp_f32 v202, v202\n\t"
      "v_exp_f32 v203, v203\n\t"
      "v_mfma_f32_32x32x16_bf16 %3, v[248:251], v[232:235], %3\n\t"
      "v_cvt_pk_bf16_f32 v228, v200, v201\n\t"
      "v_cvt_pk_bf16_f32 v229, v202, v203\n\t"
      "v_exp_f32 v204, v204\n\t"
      "v_exp_f32 v205, v205\n\t"
      "v_exp_f32 v206, v206\n\t"
      "v_exp_f32 v207, v207\n\t"
      "v_mfma_f32_32x32x16_bf16 %3, v[252:255], v[236:239], %3\n\t"
      "v_cvt_pk_bf16_f32 v230, v204, v205\n\t"
      "v_cvt_pk_bf16_f32 v231, v206, v207\n\t"
      "v_exp_f32 v208, v208\n\t"
      "v_exp_f32 v209, v209\n\t"
      "v_exp_f32 v210, v210\n\t"
      "v_exp_f32 v211, v211\n\t"
      "v_mfma_f32_32x32x16_bf16 v[192:207], v[240:243], %12, 0\n\t"
      "v_cvt_pk_bf16_f32 v232, v208, v209\n\t"
      "v_cvt_pk_bf16_f32 v233, v210, v211\n\t"
      "v_exp_f32 v212, v212\n\t"
      "v_exp_f32 v213, v213\n\t"
      "v_exp_f32 v214, v214\n\t"
      "v_exp_f32 v215, v215\n\t"
      "v_mfma_f32_32x32x16_bf16 v[192:207], v[244:247], %13, v[192:207]\n\t"
      "v_cvt_pk_bf16_f32 v234, v212, v213\n\t"
      "v_cvt_pk_bf16_f32 v235, v214, v215\n\t"
      "v_exp_f32 v216, v216\n\t"
      "v_exp_f32 v217, v217\n\t"
      "v_exp_f32 v218, v218\n\t"
      "v_exp_f32 v219, v219\n\t"
      "v_mfma_f32_32x32x16_bf16 %0, v[184:187], v[224:227], %0\n\t"
      "v_cvt_pk_bf16_f32 v236, v216, v217\n\t"
      "v_cvt_pk_bf16_f32 v237, v218, v219\n\t"
      "v_exp_f32 v220, v220\n\t"
      "v_exp_f32 v221, v221\n\t"
      "v_exp_f32 v222, v222\n\t"
      "v_exp_f32 v223, v223\n\t"
      "v_mfma_f32_32x32x16_bf16 %0, v[188:191], v[228:231], %0\n\t"
      "v_cvt_pk_bf16_f32 v238, v220, v221\n\t"
      "v_cvt_pk_bf16_f32 v239, v222, v223\n\t"
      "v_add_u32 %4, 8192, %4\n\t"
      "v_add_u32 %5, 8192, %5\n\t"
      "s_sub_u32 %6, %6, 1\n\t"
      "s_cmp_lg_u32 %6, 0\n\t"
      "s_cbranch_scc1 LTOP%=\n\t"
      "s_sub_u32 %7, %7, 1\n\t"
      "s_cmp_lg_u32 %7, 0\n\t"
      "s_cbranch_scc1 LOUTER%=\n\t"
      "v_and_b32 %4, 0x1ffff, %4\n\t"
      "v_and_b32 %5, 0x1ffff, %5\n\t"
      "s_mov_b64 vcc, -1\n\t"
      "s_branch LTOP%=\n\t"
      "LDONE%=:\n\t"
      "s_nop 1\n\t"
      "v_mfma_f32_32x32x16_bf16 %3, v[184:187], v[232:235], %3\n\t"
      "v_mfma_f32_32x32x16_bf16 %3, v[188:191], v[236:239], %3\n\t"
      "s_nop 11\n\t"
      : "+v"(o[0]), "+v"(o[1]), "+v"(o[2]), "+v"(o[3]), "+v"(kl), "+v"(vl), "=&s"(cnt), "=&s"(cch)
      : "v"(qf[0][0]), "v"(qf[0][1]), "v"(qf[1][0]), "v"(qf[1][1]), "v"(qf[2][0]), "v"(qf[2][1]), "v"(qf[3][0]), "v"(qf[3][1])
      : "memory", "scc", "vcc", "v184", "v185", "v186", "v187", "v188", "v189", "v190", "v191", "v192", "v193", "v194", "v195", "v196", "v197", "v198", "v199", "v200", "v201", "v202", "v203", "v204", "v205", "v206", "v207", "v208", "v209", "v210", "v211", "v212", "v213", "v214", "v215", "v216", "v217", "v218", "v219", "v220", "v221", "v222", "v223", "v224", "v225", "v226", "v227", "v228", "v229", "v230", "v231", "v232", "v233", "v234", "v235", "v236", "v237", "v238", "v239", "v240", "v241", "v242", "v243", "v244", "v245", "v246", "v247", "v248", "v249", "v250", "v251", "v252", "v253", "v254", "v255");
  } else {
    float m[4] = {-INFINITY, -INFINITY, -INFINITY, -INFINITY};
#pragma unroll 1
    for (int tl = 0; tl < SEQ / 32; ++tl) {
      if ((tl & (TPC - 1)) == 0) asm volatile("s_waitcnt lgkmcnt(0)\n\ts_barrier" ::: "memory");
      const unsigned boff = (tl & (NSTAGE * TPC - 1)) * BLK;
      LDSP(const char) kp = (LDSP(const char))(size_t)(kl + boff);
      LDSP(const char) vp = (LDSP(const char))(size_t)(vl + boff);
      const f16x8 k0 = kfrag(kp), k1 = kfrag(kp + 1024);
      const bf16x8 v0 = vfrag(vp), v1 = vfrag(vp + 1024);
#pragma unroll
      for (int b = 0; b < 4; ++b) {
        f32x16 s = mfma_qk(k0, __builtin_bit_cast(f16x8, qf[b][0]), (f32x16){});
        s = mfma_qk(k1, __builtin_bit_cast(f16x8, qf[b][1]), s);
        float mx = m[b];
#pragma unroll
        for (int i = 0; i < 16; ++i) { s[i] *= c; mx = fmaxf(mx, s[i]); }
        mx = swap_max(mx);
        const float al = __builtin_amdgcn_exp2f(m[b] - mx);
        m[b] = mx;
#pragma unroll
        for (int i = 0; i < 16; ++i) { s[i] = __builtin_amdgcn_exp2f(s[i] - mx); o[b][i] *= al; }
        const u32x4 pa = {pk_bf16(s[0], s[1]), pk_bf16(s[2], s[3]), pk_bf16(s[4], s[5]), pk_bf16(s[6], s[7])};
        const u32x4 pb = {pk_bf16(s[8], s[9]), pk_bf16(s[10], s[11]), pk_bf16(s[12], s[13]), pk_bf16(s[14], s[15])};
        o[b] = mfma_pv(v0, __builtin_bit_cast(bf16x8, pa), o[b]);
        o[b] = mfma_pv(v1, __builtin_bit_cast(bf16x8, pb), o[b]);
      }
    }
  }
  STAMP(2);
  char* Ow = out + hoff + (size_t)(q0 + wid * 128 + r) * ROWB + h * 8;
#pragma unroll
  for (int b = 0; b < 4; ++b) {
    auto rr = __builtin_amdgcn_permlane32_swap(__float_as_uint(o[b][12]), __float_as_uint(o[b][12]), false, false);
    const float rl = __builtin_amdgcn_rcpf(__uint_as_float(rr[1]));
    char* op = Ow + b * 32 * ROWB;
#pragma unroll
    for (int g = 0; g < 4; ++g) {
      const u32x2 w = {pk_bf16(o[b][4 * g] * rl, o[b][4 * g + 1] * rl), pk_bf16(o[b][4 * g + 2] * rl, o[b][4 * g + 3] * rl)};
      if (g < 3 || h == 0) *(u32x2*)(op + g * 16) = w;
    }
  }
  STAMP(3);
  STAMP_OUT;
}

extern "C" void kernel_launch(void* const* d_in, const int* in_sizes, int n_in, void* d_out, int out_size, void* d_ws, size_t ws_size,
                              hipStream_t stream) {
  (void)hipFuncSetAttribute((const void*)qkn_attn, hipFuncAttributeMaxDynamicSharedMemorySize, LDS_BYTES);
  qkn_attn<<<256, 512, LDS_BYTES, stream>>>((const char*)d_in[0], (const char*)d_in[1], (const char*)d_in[2],
                                            (const unsigned*)d_in[3], (char*)d_out, (unsigned long long*)d_ws);
}
```

```cpp
#include <hip/hip_runtime.h>
#include <hip/hip_bf16.h>

typedef _Float16 f16x8 __attribute__((ext_vector_type(8)));
typedef _Float16 f16x2 __attribute__((ext_vector_type(2)));
typedef __bf16 bf16x8 __attribute__((ext_vector_type(8)));
typedef __bf16 bf16x2 __attribute__((ext_vector_type(2)));
typedef short s16x4 __attribute__((ext_vector_type(4)));
typedef float f32x16 __attribute__((ext_vector_type(16)));
typedef float f32x2 __attribute__((ext_vector_type(2)));
typedef unsigned u32x4 __attribute__((ext_vector_type(4)));
typedef unsigned u32x2 __attribute__((ext_vector_type(2)));
typedef unsigned u32x4a8 __attribute__((ext_vector_type(4), aligned(8)));

#define DI __device__ __forceinline__
#define LDSP(T) __attribute__((address_space(3))) T*

constexpr int SEQ = 2048, DH = 28, NHEAD = 16, ROWB = DH * 2;
constexpr int CHUNK = 256, NCHUNK = SEQ / CHUNK, TPC = CHUNK / 32;
constexpr int BLK = 4096, STAGE = TPC * BLK, NSTAGE = 4, LDS_BYTES = NSTAGE * STAGE;
constexpr float LOG2E = 1.4426950408889634f;
constexpr float FAST_LIMIT = 24.0f;
constexpr float EPS = 1e-6f;

DI unsigned pk_bf16(float lo, float hi) { f32x2 v = {lo, hi}; return __builtin_bit_cast(unsigned, __builtin_convertvector(v, bf16x2)); }
DI unsigned pk_f16(float lo, float hi) { f32x2 v = {lo, hi}; return __builtin_bit_cast(unsigned, __builtin_convertvector(v, f16x2)); }
DI float bf_lo(unsigned w) { return __uint_as_float(w << 16); }
DI float bf_hi(unsigned w) { return __uint_as_float(w & 0xffff0000u); }
DI float dot2(unsigned a, float c) {
  return __builtin_amdgcn_fdot2_f32_bf16(__builtin_bit_cast(bf16x2, a), __builtin_bit_cast(bf16x2, a), c, false);
}
DI unsigned scale_pk(unsigned w, float r) { return pk_bf16(bf_lo(w) * r, bf_hi(w) * r); }
DI float swap_sum(float x) {
  auto rr = __builtin_amdgcn_permlane32_swap(__float_as_uint(x), __float_as_uint(x), false, false);
  return __uint_as_float(rr[0]) + __uint_as_float(rr[1]);
}
DI float swap_max(float x) {
  auto rr = __builtin_amdgcn_permlane32_swap(__float_as_uint(x), __float_as_uint(x), false, false);
  return fmaxf(__uint_as_float(rr[0]), __uint_as_float(rr[1]));
}
DI f32x16 mfma_qk(f16x8 a, f16x8 b, f32x16 c) { return __builtin_amdgcn_mfma_f32_32x32x16_f16(a, b, c, 0, 0, 0); }
DI f32x16 mfma_pv(bf16x8 a, bf16x8 b, f32x16 c) { return __builtin_amdgcn_mfma_f32_32x32x16_bf16(a, b, c, 0, 0, 0); }

struct KRaw { u32x4 a, b, c; u32x2 d; };

DI KRaw k_load(const char* p) {
  KRaw r;
  r.a = *(const u32x4a8*)(p); r.b = *(const u32x4a8*)(p + 16); r.c = *(const u32x4a8*)(p + 32); r.d = *(const u32x2*)(p + 48);
  return r;
}
DI void k_norm_store(const KRaw& k, LDSP(char) dst, unsigned pad0) {
  float s0 = dot2(k.a[0], 0.f), s1 = dot2(k.a[1], 0.f);
  s0 = dot2(k.a[2], s0); s1 = dot2(k.a[3], s1);
  s0 = dot2(k.b[0], s0); s1 = dot2(k.b[1], s1); s0 = dot2(k.b[2], s0); s1 = dot2(k.b[3], s1);
  s0 = dot2(k.c[0], s0); s1 = dot2(k.c[1], s1); s0 = dot2(k.c[2], s0); s1 = dot2(k.c[3], s1);
  s0 = dot2(k.d[0], s0); s1 = dot2(k.d[1], s1);
  const float r = __builtin_amdgcn_rcpf(__builtin_amdgcn_sqrtf(s0 + s1) + EPS);
  u32x4 o;
  o = (u32x4){scale_pk(k.a[0], r), scale_pk(k.a[1], r), scale_pk(k.a[2], r), scale_pk(k.a[3], r)};
  *(LDSP(u32x4))(dst) = o;
  o = (u32x4){scale_pk(k.b[0], r), scale_pk(k.b[1], r), scale_pk(k.b[2], r), scale_pk(k.b[3], r)};
  *(LDSP(u32x4))(dst + 512) = o;
  o = (u32x4){scale_pk(k.c[0], r), scale_pk(k.c[1], r), scale_pk(k.c[2], r), scale_pk(k.c[3], r)};
  *(LDSP(u32x4))(dst + 1024) = o;
  o = (u32x4){scale_pk(k.d[0], r), scale_pk(k.d[1], r), pad0, 0u};
  *(LDSP(u32x4))(dst + 1536) = o;
}

DI void v_dma(const char* gbase  , unsigned voff  , unsigned ldsdst  ) {
  unsigned keep; unsigned long long ex;
  asm volatile(
      "s_mov_b32 %0, m0\n\t"
      "s_mov_b64 %1, exec\n\t"
      "s_mov_b32 exec_lo, 0x3fff3fff\n\t"
      "s_mov_b32 exec_hi, 0x3fff3fff\n\t"
      "s_mov_b32 m0, %4\n\t"
      "s_nop 0\n\t"
      "global_load_lds_dword %2, %3 offset:0\n\t"    "s_add_u32 m0, m0, 32\n\t" "s_nop 0\n\t"
      "global_load_lds_dword %2, %3 offset:224\n\t"  "s_add_u32 m0, m0, 32\n\t" "s_nop 0\n\t"
      "global_load_lds_dword %2, %3 offset:448\n\t"  "s_add_u32 m0, m0, 32\n\t" "s_nop 0\n\t"
      "global_load_lds_dword %2, %3 offset:672\n\t"  "s_add_u32 m0, m0, 32\n\t" "s_nop 0\n\t"
      "global_load_lds_dword %2, %3 offset:896\n\t"  "s_add_u32 m0, m0, 32\n\t" "s_nop 0\n\t"
      "global_load_lds_dword %2, %3 offset:1120\n\t" "s_add_u32 m0, m0, 32\n\t" "s_nop 0\n\t"
      "global_load_lds_dword %2, %3 offset:1344\n\t" "s_add_u32 m0, m0, 32\n\t" "s_nop 0\n\t"
      "global_load_lds_dword %2, %3 offset:1568\n\t" "s_add_u32 m0, m0, 2080\n\t" "s_nop 0\n\t"
      "global_load_lds_dword %2, %3 offset:1792\n\t" "s_add_u32 m0, m0, 32\n\t" "s_nop 0\n\t"
      "global_load_lds_dword %2, %3 offset:2016\n\t" "s_add_u32 m0, m0, 32\n\t" "s_nop 0\n\t"
      "global_load_lds_dword %2, %3 offset:2240\n\t" "s_add_u32 m0, m0, 32\n\t" "s_nop 0\n\t"
      "global_load_lds_dword %2, %3 offset:2464\n\t" "s_add_u32 m0, m0, 32\n\t" "s_nop 0\n\t"
      "global_load_lds_dword %2, %3 offset:2688\n\t" "s_add_u32 m0, m0, 32\n\t" "s_nop 0\n\t"
      "global_load_lds_dword %2, %3 offset:2912\n\t" "s_add_u32 m0, m0, 32\n\t" "s_nop 0\n\t"
      "global_load_lds_dword %2, %3 offset:3136\n\t" "s_add_u32 m0, m0, 32\n\t" "s_nop 0\n\t"
      "global_load_lds_dword %2, %3 offset:3360\n\t"
      "s_mov_b64 exec, %1\n\t"
      "s_mov_b32 m0, %0"
      : "=&s"(keep), "=&s"(ex)
      : "v"(voff), "s"(gbase), "s"(ldsdst)
      : "memory", "scc");
}

DI s16x4 vtr(LDSP(const char) p) {
  typedef short v4s __attribute__((ext_vector_type(4)));
  return __builtin_amdgcn_ds_read_tr16_b64_v4i16((LDSP(v4s))p);
}
DI bf16x8 vfrag(LDSP(const char) p) {
  s16x4 lo = vtr(p), hi = vtr(p + 512);
  return __builtin_bit_cast(bf16x8, __builtin_shufflevector(lo, hi, 0, 1, 2, 3, 4, 5, 6, 7));
}
DI f16x8 kfrag(LDSP(const char) p) { return *(LDSP(const f16x8))p; }

#define STAMP(i)
#define STAMP_DECL
#define STAMP_OUT

__global__ __launch_bounds__(512, 2) void qkn_attn(const char* __restrict__ q, const char* __restrict__ k, const char* __restrict__ v,
                                                   const unsigned* __restrict__ t, char* __restrict__ out, unsigned long long* ws) {
  extern __shared__ __attribute__((aligned(16))) char smem[];
  STAMP_DECL;
  STAMP(0);
  const int bid = blockIdx.x, x = bid & 7, y = bid >> 3;
  const int head = x * 8 + (y >> 2), q0 = (y & 3) * 512;
  const size_t hoff = (size_t)head * SEQ * ROWB;
  LDSP(char) lds = (LDSP(char))smem;
  const unsigned lds0 = (unsigned)(size_t)lds;
  const int tid = threadIdx.x, lane = tid & 63, r = lane & 31, h = lane >> 5;
  const int wid = __builtin_amdgcn_readfirstlane(tid >> 6);
  const unsigned tw = t[(head & (NHEAD - 1)) >> 1];
  const float c = __uint_as_float((head & 1) ? (tw & 0xffff0000u) : (tw << 16)) * LOG2E;
  const bool fast = fabsf(c) <= FAST_LIMIT;

  if (wid >= 4) {
    const int pw = wid - 4, ptid = tid - 256;
    const char* Kh = k + hoff + (size_t)ptid * ROWB;
    const char* Vh = v + hoff + (size_t)pw * 64 * ROWB;
    const unsigned voff = (unsigned)((lane >> 4) * ROWB + (lane & 15) * 4);
    const unsigned vdst = lds0 + pw * 2 * BLK + 2048;
    LDSP(char) kdst = lds + (ptid >> 5) * BLK + (ptid & 31) * 16;
    KRaw ka = k_load(Kh);
    KRaw kb = k_load(Kh + CHUNK * ROWB);
    v_dma(Vh, voff, vdst);
    {
      const u32x2 one = {0x00003F80u, 0u};
#pragma unroll
      for (int s = 0; s < NSTAGE; ++s) *(LDSP(u32x2))(lds + s * STAGE + (ptid >> 5) * BLK + 2048 + (ptid & 31) * 64 + 56) = one;
    }
    const unsigned kpad = fast ? pk_bf16(-fabsf(c), 0.f) : 0u;
    k_norm_store(ka, kdst, kpad);
    v_dma(Vh + CHUNK * ROWB, voff, vdst + STAGE);
    asm volatile("s_waitcnt vmcnt(16) lgkmcnt(0)\n\ts_barrier" ::: "memory");
#pragma unroll 1
    for (int ch = 1; ch < NCHUNK; ++ch) {
      __builtin_amdgcn_s_waitcnt(0x0F70);
      ka = kb;
      if (ch + 1 < NCHUNK) {
        kb = k_load(Kh + (size_t)(ch + 1) * CHUNK * ROWB);
        v_dma(Vh + (size_t)(ch + 1) * CHUNK * ROWB, voff, vdst + ((ch + 1) & (NSTAGE - 1)) * STAGE);
      }
      k_norm_store(ka, kdst + (ch & (NSTAGE - 1)) * STAGE, kpad);
      asm volatile("s_waitcnt lgkmcnt(0)\n\ts_barrier" ::: "memory");
    }
    return;
  }

  const char* Qw = q + hoff + (size_t)(q0 + wid * 128 + r) * ROWB + h * 16;
  u32x4 qa[4]; u32x2 qb[4], qc[4];
#pragma unroll
  for (int b = 0; b < 4; ++b) {
    const char* qp = Qw + b * 32 * ROWB;
    qa[b] = *(const u32x4a8*)qp;
    qb[b] = *(const u32x2*)(qp + 32);
    qc[b] = *(const u32x2*)(qp + (h ? 32 : 40));
  }
  u32x4 qf[4][2];
  const unsigned hmask = h ? 0u : 0xffffffffu;
  const float qscale = fast ? c : 1.0f;
  const unsigned qpad = h ? 0x00003F80u : 0u;
#pragma unroll
  for (int b = 0; b < 4; ++b) {
    qc[b][0] &= hmask; qc[b][1] &= hmask;
    float s0 = dot2(qa[b][0], 0.f), s1 = dot2(qa[b][1], 0.f);
    s0 = dot2(qa[b][2], s0); s1 = dot2(qa[b][3], s1);
    s0 = dot2(qb[b][0], s0); s1 = dot2(qb[b][1], s1);
    s0 = dot2(qc[b][0], s0); s1 = dot2(qc[b][1], s1);
    const float ss = swap_sum(s0 + s1);
    const float rq = qscale * __builtin_amdgcn_rcpf(__builtin_amdgcn_sqrtf(ss) + EPS);
    qf[b][0] = (u32x4){scale_pk(qa[b][0], rq), scale_pk(qa[b][1], rq), scale_pk(qa[b][2], rq), scale_pk(qa[b][3], rq)};
    qf[b][1] = (u32x4){scale_pk(qb[b][0], rq), scale_pk(qb[b][1], rq), scale_pk(qc[b][0], rq) | qpad, scale_pk(qc[b][1], rq)};
  }
  f32x16 o[4] = {};
  unsigned kl = lds0 + h * 512 + r * 16;
  unsigned vl = lds0 + 2048 + (4 * h + ((lane & 15) >> 2)) * 64 + ((lane >> 4) & 1) * 32 + (lane & 3) * 8;
  STAMP(1);
  if (fast) {
    unsigned cnt, cch;
    asm volatile(
      "s_setprio 3\n\t"
      "v_mov_b32 v232, 0\n\t"
      "v_mov_b32 v233, 0\n\t"
      "v_mov_b32 v234, 0\n\t"
      "v_mov_b32 v235, 0\n\t"
      "v_mov_b32 v236, 0\n\t"
      "v_mov_b32 v237, 0\n\t"
      "v_mov_b32 v238, 0\n\t"
      "v_mov_b32 v239, 0\n\t"
      "v_mov_b32 v184, 0\n\t"
      "v_mov_b32 v185, 0\n\t"
      "v_mov_b32 v186, 0\n\t"
      "v_mov_b32 v187, 0\n\t"
      "v_mov_b32 v188, 0\n\t"
      "v_mov_b32 v189, 0\n\t"
      "v_mov_b32 v190, 0\n\t"
      "v_mov_b32 v191, 0\n\t"
      "s_mov_b64 vcc, 0\n\t"
      "s_barrier\n\t"
      "ds_read_b128 v[240:243], %4 offset:0\n\t"
      "ds_read_b128 v[244:247], %4 offset:1024\n\t"
      "ds_read_b64_tr_b16 v[248:249], %5 offset:0\n\t"
      "ds_read_b64_tr_b16 v[250:251], %5 offset:512\n\t"
      "ds_read_b64_tr_b16 v[252:253], %5 offset:1024\n\t"
      "ds_read_b64_tr_b16 v[254:255], %5 offset:1536\n\t"
      "s_mov_b32 %6, 4\n\t"
      "s_mov_b32 %7, 8\n\t"
      "s_waitcnt lgkmcnt(4)\n\t"
      "v_mfma_f32_32x32x16_bf16 v[192:207], v[240:243], %8, 0\n\t"
      "v_mfma_f32_32x32x16_bf16 v[192:207], v[244:247], %9, v[192:207]\n\t"
      "s_waitcnt lgkmcnt(0)\n\t"
      "s_nop 10\n\t"
      "s_branch LMID%=\n\t"
      "LOUTER%=:\n\t"
      "s_barrier\n\t"
      "v_and_b32 %4, 0x1ffff, %4\n\t"
      "v_and_b32 %5, 0x1ffff, %5\n\t"
      "s_mov_b32 %6, 4\n\t"
      "LTOP%=:\n\t"
      "v_exp_f32 v192, v192\n\t"
      "v_exp_f32 v193, v193\n\t"
      "v_exp_f32 v194, v194\n\t"
      "v_exp_f32 v195, v195\n\t"
      "v_mfma_f32_32x32x16_bf16 v[208:223], v[240:243], %14, 0\n\t"
      "ds_read_b128 v[240:243], %4 offset:0\n\t"
      "ds_read_b64_tr_b16 v[248:249], %5 offset:0\n\t"
      "v_cvt_pk_bf16_f32 v224, v192, v193\n\t"
      "v_cvt_pk_bf16_f32 v225, v194, v195\n\t"
      "v_exp_f32 v196, v196\n\t"
      "v_exp_f32 v197, v197\n\t"
      "v_exp_f32 v198, v198\n\t"
      "v_exp_f32 v199, v199\n\t"
      "v_mfma_f32_32x32x16_bf16 v[208:223], v[244:247], %15, v[208:223]\n\t"
      "ds_read_b128 v[244:247], %4 offset:1024\n\t"
      "ds_read_b64_tr_b16 v[250:251], %5 offset:512\n\t"
      "v_cvt_pk_bf16_f32 v226, v196, v197\n\t"
      "v_cvt_pk_bf16_f32 v227, v198, v199\n\t"
      "v_exp_f32 v200, v200\n\t"
      "v_exp_f32 v201, v201\n\t"
      "v_exp_f32 v202, v202\n\t"
      "v_exp_f32 v203, v203\n\t"
      "v_mfma_f32_32x32x16_bf16 %1, v[184:187], v[232:235], %1\n\t"
      "ds_read_b64_tr_b16 v[252:253], %5 offset:1024\n\t"
      "v_cvt_pk_bf16_f32 v228, v200, v201\n\t"
      "v_cvt_pk_bf16_f32 v229, v202, v203\n\t"
      "v_exp_f32 v204, v204\n\t"
      "v_exp_f32 v205, v205\n\t"
      "v_exp_f32 v206, v206\n\t"
      "v_exp_f32 v207, v207\n\t"
      "v_mfma_f32_32x32x16_bf16 %1, v[188:191], v[236:239], %1\n\t"
      "ds_read_b64_tr_b16 v[254:255], %5 offset:1536\n\t"
      "v_cvt_pk_bf16_f32 v230, v204, v205\n\t"
      "v_cvt_pk_bf16_f32 v231, v206, v207\n\t"
      "v_exp_f32 v208, v208\n\t"
      "v_exp_f32 v209, v209\n\t"
      "v_exp_f32 v210, v210\n\t"
      "v_exp_f32 v211, v211\n\t"
      "s_waitcnt lgkmcnt(0)\n\t"
      "v_mfma_f32_32x32x16_bf16 v[192:207], v[240:243], %8, 0\n\t"
      "v_cvt_pk_bf16_f32 v232, v208, v209\n\t"
      "v_cvt_pk_bf16_f32 v233, v210, v211\n\t"
      "v_exp_f32 v212, v212\n\t"
      "v_exp_f32 v213, v213\n\t"
      "v_exp_f32 v214, v214\n\t"
      "v_exp_f32 v215, v215\n\t"
      "v_mfma_f32_32x32x16_bf16 v[192:207], v[244:247], %9, v[192:207]\n\t"
      "v_cvt_pk_bf16_f32 v234, v212, v213\n\t"
      "v_cvt_pk_bf16_f32 v235, v214, v215\n\t"
      "v_exp_f32 v216, v216\n\t"
      "v_exp_f32 v217, v217\n\t"
      "v_exp_f32 v218, v218\n\t"
      "v_exp_f32 v219, v219\n\t"
      "v_mfma_f32_32x32x16_bf16 %2, v[184:187], v[224:227], %2\n\t"
      "v_cvt_pk_bf16_f32 v236, v216, v217\n\t"
      "v_cvt_pk_bf16_f32 v237, v218, v219\n\t"
      "v_exp_f32 v220, v220\n\t"
      "v_exp_f32 v221, v221\n\t"
      "v_exp_f32 v222, v222\n\t"
      "v_exp_f32 v223, v223\n\t"
      "v_mfma_f32_32x32x16_bf16 %2, v[188:191], v[228:231], %2\n\t"
      "v_cvt_pk_bf16_f32 v238, v220, v221\n\t"
      "v_cvt_pk_bf16_f32 v239, v222, v223\n\t"
      "s_cbranch_vccnz LDONE%=\n\t"
      "LMID%=:\n\t"
      "v_exp_f32 v192, v192\n\t"
      "v_exp_f32 v193, v193\n\t"
      "v_exp_f32 v194, v194\n\t"
      "v_exp_f32 v195, v195\n\t"
      "v_mfma_f32_32x32x16_bf16 v[208:223], v[240:243], %10, 0\n\t"
      "v_cvt_pk_bf16_f32 v224, v192, v193\n\t"
      "v_cvt_pk_bf16_f32 v225, v194, v195\n\t"
      "v_exp_f32 v196, v196\n\t"
      "v_exp_f32 v197, v197\n\t"
      "v_exp_f32 v198, v198\n\t"
      "v_exp_f32 v199, v199\n\t"
      "v_mfma_f32_32x32x16_bf16 v[208:223], v[244:247], %11, v[208:223]\n\t"
      "v_cvt_pk_bf16_f32 v226, v196, v197\n\t"
      "v_cvt_pk_bf16_f32 v227, v198, v199\n\t"
      "v_exp_f32 v200, v200\n\t"
      "v_exp_f32 v201, v201\n\t"
      "v_exp_f32 v202, v202\n\t"
      "v_exp_f32 v203, v203\n\t"
      "v_mfma_f32_32x32x16_bf16 %3, v[184:187], v[232:235], %3\n\t"
      "v_cvt_pk_bf16_f32 v228, v200, v201\n\t"
      "v_cvt_pk_bf16_f32 v229, v202, v203\n\t"
      "v_exp_f32 v204, v204\n\t"
      "v_exp_f32 v205, v205\n\t"
      "v_exp_f32 v206, v206\n\t"
      "v_exp_f32 v207, v207\n\t"
      "v_mfma_f32_32x32x16_bf16 %3, v[188:191], v[236:239], %3\n\t"
      "v_cvt_pk_bf16_f32 v230, v204, v205\n\t"
      "v_cvt_pk_bf16_f32 v231, v206, v207\n\t"
      "v_exp_f32 v208, v208\n\t"
      "v_exp_f32 v209, v209\n\t"
      "v_exp_f32 v210, v210\n\t"
      "v_exp_f32 v211, v211\n\t"
      "v_mfma_f32_32x32x16_bf16 v[192:207], v[240:243], %12, 0\n\t"
      "v_cvt_pk_bf16_f32 v232, v208, v209\n\t"
      "v_cvt_pk_bf16_f32 v233, v210, v211\n\t"
      "v_exp_f32 v212, v212\n\t"
      "v_exp_f32 v213, v213\n\t"
      "v_exp_f32 v214, v214\n\t"
      "v_exp_f32 v215, v215\n\t"
      "v_mfma_f32_32x32x16_bf16 v[192:207], v[244:247], %13, v[192:207]\n\t"
      "v_cvt_pk_bf16_f32 v234, v212, v213\n\t"
      "v_cvt_pk_bf16_f32 v235, v214, v215\n\t"
      "v_exp_f32 v216, v216\n\t"
      "v_exp_f32 v217, v217\n\t"
      "v_exp_f32 v218, v218\n\t"
      "v_exp_f32 v219, v219\n\t"
      "v_mfma_f32_32x32x16_bf16 %0, v[248:251], v[224:227], %0\n\t"
      "v_cvt_pk_bf16_f32 v236, v216, v217\n\t"
      "v_cvt_pk_bf16_f32 v237, v218, v219\n\t"
      "v_exp_f32 v220, v220\n\t"
      "v_exp_f32 v221, v221\n\t"
      "v_exp_f32 v222, v222\n\t"
      "v_exp_f32 v223, v223\n\t"
      "v_mfma_f32_32x32x16_bf16 %0, v[252:255], v[228:231], %0\n\t"
      "v_cvt_pk_bf16_f32 v238, v220, v221\n\t"
      "v_cvt_pk_bf16_f32 v239, v222, v223\n\t"
      "v_exp_f32 v192, v192\n\t"
      "v_exp_f32 v193, v193\n\t"
      "v_exp_f32 v194, v194\n\t"
      "v_exp_f32 v195, v195\n\t"
      "v_mfma_f32_32x32x16_bf16 v[208:223], v[240:243], %14, 0\n\t"
      "ds_read_b128 v[240:243], %4 offset:4096\n\t"
      "ds_read_b64_tr_b16 v[184:185], %5 offset:4096\n\t"
      "v_cvt_pk_bf16_f32 v224, v192, v193\n\t"
      "v_cvt_pk_bf16_f32 v225, v194, v195\n\t"
      "v_exp_f32 v196, v196\n\t"
      "v_exp_f32 v197, v197\n\t"
      "v_exp_f32 v198, v198\n\t"
      "v_exp_f32 v199, v199\n\t"
      "v_mfma_f32_32x32x16_bf16 v[208:223], v[244:247], %15, v[208:223]\n\t"
      "ds_read_b128 v[244:247], %4 offset:5120\n\t"
      "ds_read_b64_tr_b16 v[186:187], %5 offset:4608\n\t"
      "v_cvt_pk_bf16_f32 v226, v196, v197\n\t"
      "v_cvt_pk_bf16_f32 v227, v198, v199\n\t"
      "v_exp_f32 v200, v200\n\t"
      "v_exp_f32 v201, v201\n\t"
      "v_exp_f32 v202, v202\n\t"
      "v_exp_f32 v203, v203\n\t"
      "v_mfma_f32_32x32x16_bf16 %1, v[248:251], v[232:235], %1\n\t"
      "ds_read_b64_tr_b16 v[188:189], %5 offset:5120\n\t"
      "v_cvt_pk_bf16_f32 v228, v200, v201\n\t"
      "v_cvt_pk_bf16_f32 v229, v202, v203\n\t"
      "v_exp_f32 v204, v204\n\t"
      "v_exp_f32 v205, v205\n\t"
      "v_exp_f32 v206, v206\n\t"
      "v_exp_f32 v207, v207\n\t"
      "v_mfma_f32_32x32x16_bf16 %1, v[252:255], v[236:239], %1\n\t"
      "ds_read_b64_tr_b16 v[190:191], %5 offset:5632\n\t"
      "v_cvt_pk_bf16_f32 v230, v204, v205\n\t"
      "v_cvt_pk_bf16_f32 v231, v206, v207\n\t"
      "v_exp_f32 v208, v208\n\t"
      "v_exp_f32 v209, v209\n\t"
      "v_exp_f32 v210, v210\n\t"
      "v_exp_f32 v211, v211\n\t"
      "s_waitcnt lgkmcnt(0)\n\t"
      "v_mfma_f32_32x32x16_bf16 v[192:207], v[240:243], %8, 0\n\t"
      "v_cvt_pk_bf16_f32 v232, v208, v209\n\t"
      "v_cvt_pk_bf16_f32 v233, v210, v211\n\t"
      "v_exp_f32 v212, v212\n\t"
      "v_exp_f32 v213, v213\n\t"
      "v_exp_f32 v214, v214\n\t"
      "v_exp_f32 v215, v215\n\t"
      "v_mfma_f32_32x32x16_bf16 v[192:207], v[244:247], %9, v[192:207]\n\t"
      "v_cvt_pk_bf16_f32 v234, v212, v213\n\t"
      "v_cvt_pk_bf16_f32 v235, v214, v215\n\t"
      "v_exp_f32 v216, v216\n\t"
      "v_exp_f32 v217, v217\n\t"
      "v_exp_f32 v218, v218\n\t"
      "v_exp_f32 v219, v219\n\t"
      "v_mfma_f32_32x32x16_bf16 %2, v[248:251], v[224:227], %2\n\t"
      "v_cvt_pk_bf16_f32 v236, v216, v217\n\t"
      "v_cvt_pk_bf16_f32 v237, v218, v219\n\t"
      "v_exp_f32 v220, v220\n\t"
      "v_exp_f32 v221, v221\n\t"
      "v_exp_f32 v222, v222\n\t"
      "v_exp_f32 v223, v223\n\t"
      "v_mfma_f32_32x32x16_bf16 %2, v[252:255], v[228:231], %2\n\t"
      "v_cvt_pk_bf16_f32 v238, v220, v221\n\t"
      "v_cvt_pk_bf16_f32 v239, v222, v223\n\t"
      "v_exp_f32 v192, v192\n\t"
      "v_exp_f32 v193, v193\n\t"
      "v_exp_f32 v194, v194\n\t"
      "v_exp_f32 v195, v195\n\t"
      "v_mfma_f32_32x32x16_bf16 v[208:223], v[240:243], %10, 0\n\t"
      "v_cvt_pk_bf16_f32 v224, v192, v193\n\t"
      "v_cvt_pk_bf16_f32 v225, v194, v195\n\t"
      "v_exp_f32 v196, v196\n\t"
      "v_exp_f32 v197, v197\n\t"
      "v_exp_f32 v198, v198\n\t"
      "v_exp_f32 v199, v199\n\t"
      "v_mfma_f32_32x32x16_bf16 v[208:223], v[244:247], %11, v[208:223]\n\t"
      "v_cvt_pk_bf16_f32 v226, v196, v197\n\t"
      "v_cvt_pk_bf16_f32 v227, v198, v199\n\t"
      "v_exp_f32 v200, v200\n\t"
      "v_exp_f32 v201, v201\n\t"
      "v_exp_f32 v202, v202\n\t"
      "v_exp_f32 v203, v203\n\t"
      "v_mfma_f32_32x32x16_bf16 %3, v[248:251], v[232:235], %3\n\t"
      "v_cvt_pk_bf16_f32 v228, v200, v201\n\t"
      "v_cvt_pk_bf16_f32 v229, v202, v203\n\t"
      "v_exp_f32 v204, v204\n\t"
      "v_exp_f32 v205, v205\n\t"
      "v_exp_f32 v206, v206\n\t"
      "v_exp_f32 v207, v207\n\t"
      "v_mfma_f32_32x32x16_bf16 %3, v[252:255], v[236:239], %3\n\t"
      "v_cvt_pk_bf16_f32 v230, v204, v205\n\t"
      "v_cvt_pk_bf16_f32 v231, v206, v207\n\t"
      "v_exp_f32 v208, v208\n\t"
      "v_exp_f32 v209, v209\n\t"
      "v_exp_f32 v210, v210\n\t"
      "v_exp_f32 v211, v211\n\t"
      "v_mfma_f32_32x32x16_bf16 v[192:207], v[240:243], %12, 0\n\t"
      "v_cvt_pk_bf16_f32 v232, v208, v209\n\t"
      "v_cvt_pk_bf16_f32 v233, v210, v211\n\t"
      "v_exp_f32 v212, v212\n\t"
      "v_exp_f32 v213, v213\n\t"
      "v_exp_f32 v214, v214\n\t"
      "v_exp_f32 v215, v215\n\t"
      "v_mfma_f32_32x32x16_bf16 v[192:207], v[244:247], %13, v[192:207]\n\t"
      "v_cvt_pk_bf16_f32 v234, v212, v213\n\t"
      "v_cvt_pk_bf16_f32 v235, v214, v215\n\t"
      "v_exp_f32 v216, v216\n\t"
      "v_exp_f32 v217, v217\n\t"
      "v_exp_f32 v218, v218\n\t"
      "v_exp_f32 v219, v219\n\t"
      "v_mfma_f32_32x32x16_bf16 %0, v[184:187], v[224:227], %0\n\t"
      "v_cvt_pk_bf16_f32 v236, v216, v217\n\t"
      "v_cvt_pk_bf16_f32 v237, v218, v219\n\t"
      "v_exp_f32 v220, v220\n\t"
      "v_exp_f32 v221, v221\n\t"
      "v_exp_f32 v222, v222\n\t"
      "v_exp_f32 v223, v223\n\t"
      "v_mfma_f32_32x32x16_bf16 %0, v[188:191], v[228:231], %0\n\t"
      "v_cvt_pk_bf16_f32 v238, v220, v221\n\t"
      "v_cvt_pk_bf16_f32 v239, v222, v223\n\t"
      "v_add_u32 %4, 8192, %4\n\t"
      "v_add_u32 %5, 8192, %5\n\t"
      "s_sub_u32 %6, %6, 1\n\t"
      "s_cmp_lg_u32 %6, 0\n\t"
      "s_cbranch_scc1 LTOP%=\n\t"
      "s_sub_u32 %7, %7, 1\n\t"
      "s_cmp_lg_u32 %7, 0\n\t"
      "s_cbranch_scc1 LOUTER%=\n\t"
      "v_and_b32 %4, 0x1ffff, %4\n\t"
      "v_and_b32 %5, 0x1ffff, %5\n\t"
      "s_mov_b64 vcc, -1\n\t"
      "s_branch LTOP%=\n\t"
      "LDONE%=:\n\t"
      "s_nop 1\n\t"
      "v_mfma_f32_32x32x16_bf16 %3, v[184:187], v[232:235], %3\n\t"
      "v_mfma_f32_32x32x16_bf16 %3, v[188:191], v[236:239], %3\n\t"
      "s_nop 11\n\t"
      : "+v"(o[0]), "+v"(o[1]), "+v"(o[2]), "+v"(o[3]), "+v"(kl), "+v"(vl), "=&s"(cnt), "=&s"(cch)
      : "v"(qf[0][0]), "v"(qf[0][1]), "v"(qf[1][0]), "v"(qf[1][1]), "v"(qf[2][0]), "v"(qf[2][1]), "v"(qf[3][0]), "v"(qf[3][1])
      : "memory", "scc", "vcc", "v184", "v185", "v186", "v187", "v188", "v189", "v190", "v191", "v192", "v193", "v194", "v195", "v196", "v197", "v198", "v199", "v200", "v201", "v202", "v203", "v204", "v205", "v206", "v207", "v208", "v209", "v210", "v211", "v212", "v213", "v214", "v215", "v216", "v217", "v218", "v219", "v220", "v221", "v222", "v223", "v224", "v225", "v226", "v227", "v228", "v229", "v230", "v231", "v232", "v233", "v234", "v235", "v236", "v237", "v238", "v239", "v240", "v241", "v242", "v243", "v244", "v245", "v246", "v247", "v248", "v249", "v250", "v251", "v252", "v253", "v254", "v255");
  } else {
    float m[4] = {-INFINITY, -INFINITY, -INFINITY, -INFINITY};
#pragma unroll 1
    for (int tl = 0; tl < SEQ / 32; ++tl) {
      if ((tl & (TPC - 1)) == 0) asm volatile("s_waitcnt lgkmcnt(0)\n\ts_barrier" ::: "memory");
      const unsigned boff = (tl & (NSTAGE * TPC - 1)) * BLK;
      LDSP(const char) kp = (LDSP(const char))(size_t)(kl + boff);
      LDSP(const char) vp = (LDSP(const char))(size_t)(vl + boff);
      const f16x8 k0 = kfrag(kp), k1 = kfrag(kp + 1024);
      const bf16x8 v0 = vfrag(vp), v1 = vfrag(vp + 1024);
#pragma unroll
      for (int b = 0; b < 4; ++b) {
        f32x16 s = mfma_qk(k0, __builtin_bit_cast(f16x8, qf[b][0]), (f32x16){});
        s = mfma_qk(k1, __builtin_bit_cast(f16x8, qf[b][1]), s);
        float mx = m[b];
#pragma unroll
        for (int i = 0; i < 16; ++i) { s[i] *= c; mx = fmaxf(mx, s[i]); }
        mx = swap_max(mx);
        const float al = __builtin_amdgcn_exp2f(m[b] - mx);
        m[b] = mx;
#pragma unroll
        for (int i = 0; i < 16; ++i) { s[i] = __builtin_amdgcn_exp2f(s[i] - mx); o[b][i] *= al; }
        const u32x4 pa = {pk_bf16(s[0], s[1]), pk_bf16(s[2], s[3]), pk_bf16(s[4], s[5]), pk_bf16(s[6], s[7])};
        const u32x4 pb = {pk_bf16(s[8], s[9]), pk_bf16(s[10], s[11]), pk_bf16(s[12], s[13]), pk_bf16(s[14], s[15])};
        o[b] = mfma_pv(v0, __builtin_bit_cast(bf16x8, pa), o[b]);
        o[b] = mfma_pv(v1, __builtin_bit_cast(bf16x8, pb), o[b]);
      }
    }
  }
  STAMP(2);
  LDSP(char) stg = lds + wid * 8192;
#pragma unroll
  for (int b = 0; b < 4; ++b) {
    auto rr = __builtin_amdgcn_permlane32_swap(__float_as_uint(o[b][12]), __float_as_uint(o[b][12]), false, false);
    const float rl = __builtin_amdgcn_rcpf(__uint_as_float(rr[1]));
    LDSP(char) sp = stg + (b * 32 + r) * ROWB + h * 8;
#pragma unroll
    for (int g = 0; g < 4; ++g) {
      const u32x2 w = {pk_bf16(o[b][4 * g] * rl, o[b][4 * g + 1] * rl), pk_bf16(o[b][4 * g + 2] * rl, o[b][4 * g + 3] * rl)};
      if (g < 3 || h == 0) *(LDSP(u32x2))(sp + g * 16) = w;
    }
  }
  {
    char* Ow = out + hoff + (size_t)(q0 + wid * 128) * ROWB + 3072;
    const unsigned lo = lane * 16;
#pragma unroll
    for (int i = 0; i < 7; ++i) {
      const u32x4 w = *(LDSP(u32x4))(stg + i * 1024 + lo);
      asm volatile("global_store_dwordx4 %0, %1, %2 offset:%3\n\ts_nop 1" :: "v"(lo), "v"(w), "s"(Ow), "i"(i * 1024 - 3072) : "memory");
    }
  }
  STAMP(3);
  STAMP_OUT;
}

extern "C" void kernel_launch(void* const* d_in, const int* in_sizes, int n_in, void* d_out, int out_size, void* d_ws, size_t ws_size,
                              hipStream_t stream) {
  (void)hipFuncSetAttribute((const void*)qkn_attn, hipFuncAttributeMaxDynamicSharedMemorySize, LDS_BYTES);
  qkn_attn<<<256, 512, LDS_BYTES, stream>>>((const char*)d_in[0], (const char*)d_in[1], (const char*)d_in[2],
                                            (const unsigned*)d_in[3], (char*)d_out, (unsigned long long*)d_ws);
}
```

```cpp
#include <hip/hip_runtime.h>
#include <hip/hip_bf16.h>

typedef _Float16 f16x8 __attribute__((ext_vector_type(8)));
typedef _Float16 f16x2 __attribute__((ext_vector_type(2)));
typedef __bf16 bf16x8 __attribute__((ext_vector_type(8)));
typedef __bf16 bf16x2 __attribute__((ext_vector_type(2)));
typedef short s16x4 __attribute__((ext_vector_type(4)));
typedef float f32x16 __attribute__((ext_vector_type(16)));
typedef float f32x2 __attribute__((ext_vector_type(2)));
typedef unsigned u32x4 __attribute__((ext_vector_type(4)));
typedef unsigned u32x2 __attribute__((ext_vector_type(2)));
typedef unsigned u32x4a8 __attribute__((ext_vector_type(4), aligned(8)));

#define DI __device__ __forceinline__
#define LDSP(T) __attribute__((address_space(3))) T*

constexpr int SEQ = 2048, DH = 28, NHEAD = 16, ROWB = DH * 2;
constexpr int CHUNK = 256, NCHUNK = SEQ / CHUNK, TPC = CHUNK / 32;
constexpr int BLK = 4096, STAGE = TPC * BLK, NSTAGE = 4, LDS_BYTES = NSTAGE * STAGE;
constexpr float LOG2E = 1.4426950408889634f;
constexpr float FAST_LIMIT = 24.0f;
constexpr float EPS = 1e-6f;

DI unsigned pk_bf16(float lo, float hi) { f32x2 v = {lo, hi}; return __builtin_bit_cast(unsigned, __builtin_convertvector(v, bf16x2)); }
DI unsigned pk_f16(float lo, float hi) { f32x2 v = {lo, hi}; return __builtin_bit_cast(unsigned, __builtin_convertvector(v, f16x2)); }
DI float bf_lo(unsigned w) { return __uint_as_float(w << 16); }
DI float bf_hi(unsigned w) { return __uint_as_float(w & 0xffff0000u); }
DI float dot2(unsigned a, float c) {
  return __builtin_amdgcn_fdot2_f32_bf16(__builtin_bit_cast(bf16x2, a), __builtin_bit_cast(bf16x2, a), c, false);
}
DI unsigned scale_pk(unsigned w, float r) { return pk_bf16(bf_lo(w) * r, bf_hi(w) * r); }
DI float swap_sum(float x) {
  auto rr = __builtin_amdgcn_permlane32_swap(__float_as_uint(x), __float_as_uint(x), false, false);
  return __uint_as_float(rr[0]) + __uint_as_float(rr[1]);
}
DI float swap_max(float x) {
  auto rr = __builtin_amdgcn_permlane32_swap(__float_as_uint(x), __float_as_uint(x), false, false);
  return fmaxf(__uint_as_float(rr[0]), __uint_as_float(rr[1]));
}
DI f32x16 mfma_qk(f16x8 a, f16x8 b, f32x16 c) { return __builtin_amdgcn_mfma_f32_32x32x16_f16(a, b, c, 0, 0, 0); }
DI f32x16 mfma_pv(bf16x8 a, bf16x8 b, f32x16 c) { return __builtin_amdgcn_mfma_f32_32x32x16_bf16(a, b, c, 0, 0, 0); }

struct KRaw { u32x4 a, b, c; u32x2 d; };

DI KRaw k_load(const char* p) {
  KRaw r;
  r.a = *(const u32x4a8*)(p); r.b = *(const u32x4a8*)(p + 16); r.c = *(const u32x4a8*)(p + 32); r.d = *(const u32x2*)(p + 48);
  return r;
}
DI void k_norm_store(const KRaw& k, LDSP(char) dst, unsigned pad0) {
  float s0 = dot2(k.a[0], 0.f), s1 = dot2(k.a[1], 0.f);
  s0 = dot2(k.a[2], s0); s1 = dot2(k.a[3], s1);
  s0 = dot2(k.b[0], s0); s1 = dot2(k.b[1], s1); s0 = dot2(k.b[2], s0); s1 = dot2(k.b[3], s1);
  s0 = dot2(k.c[0], s0); s1 = dot2(k.c[1], s1); s0 = dot2(k.c[2], s0); s1 = dot2(k.c[3], s1);
  s0 = dot2(k.d[0], s0); s1 = dot2(k.d[1], s1);
  const float r = __builtin_amdgcn_rcpf(__builtin_amdgcn_sqrtf(s0 + s1) + EPS);
  u32x4 o;
  o = (u32x4){scale_pk(k.a[0], r), scale_pk(k.a[1], r), scale_pk(k.a[2], r), scale_pk(k.a[3], r)};
  *(LDSP(u32x4))(dst) = o;
  o = (u32x4){scale_pk(k.b[0], r), scale_pk(k.b[1], r), scale_pk(k.b[2], r), scale_pk(k.b[3], r)};
  *(LDSP(u32x4))(dst + 512) = o;
  o = (u32x4){scale_pk(k.c[0], r), scale_pk(k.c[1], r), scale_pk(k.c[2], r), scale_pk(k.c[3], r)};
  *(LDSP(u32x4))(dst + 1024) = o;
  o = (u32x4){scale_pk(k.d[0], r), scale_pk(k.d[1], r), pad0, 0u};
  *(LDSP(u32x4))(dst + 1536) = o;
}

DI void v_dma(const char* gbase  , unsigned voff  , unsigned ldsdst  ) {
  unsigned keep; unsigned long long ex;
  asm volatile(
      "s_mov_b32 %0, m0\n\t"
      "s_mov_b64 %1, exec\n\t"
      "s_mov_b32 exec_lo, 0x3fff3fff\n\t"
      "s_mov_b32 exec_hi, 0x3fff3fff\n\t"
      "s_mov_b32 m0, %4\n\t"
      "s_nop 0\n\t"
      "global_load_lds_dword %2, %3 offset:0\n\t"    "s_add_u32 m0, m0, 32\n\t" "s_nop 0\n\t"
      "global_load_lds_dword %2, %3 offset:224\n\t"  "s_add_u32 m0, m0, 32\n\t" "s_nop 0\n\t"
      "global_load_lds_dword %2, %3 offset:448\n\t"  "s_add_u32 m0, m0, 32\n\t" "s_nop 0\n\t"
      "global_load_lds_dword %2, %3 offset:672\n\t"  "s_add_u32 m0, m0, 32\n\t" "s_nop 0\n\t"
      "global_load_lds_dword %2, %3 offset:896\n\t"  "s_add_u32 m0, m0, 32\n\t" "s_nop 0\n\t"
      "global_load_lds_dword %2, %3 offset:1120\n\t" "s_add_u32 m0, m0, 32\n\t" "s_nop 0\n\t"
      "global_load_lds_dword %2, %3 offset:1344\n\t" "s_add_u32 m0, m0, 32\n\t" "s_nop 0\n\t"
      "global_load_lds_dword %2, %3 offset:1568\n\t" "s_add_u32 m0, m0, 2080\n\t" "s_nop 0\n\t"
      "global_load_lds_dword %2, %3 offset:1792\n\t" "s_add_u32 m0, m0, 32\n\t" "s_nop 0\n\t"
      "global_load_lds_dword %2, %3 offset:2016\n\t" "s_add_u32 m0, m0, 32\n\t" "s_nop 0\n\t"
      "global_load_lds_dword %2, %3 offset:2240\n\t" "s_add_u32 m0, m0, 32\n\t" "s_nop 0\n\t"
      "global_load_lds_dword %2, %3 offset:2464\n\t" "s_add_u32 m0, m0, 32\n\t" "s_nop 0\n\t"
      "global_load_lds_dword %2, %3 offset:2688\n\t" "s_add_u32 m0, m0, 32\n\t" "s_nop 0\n\t"
      "global_load_lds_dword %2, %3 offset:2912\n\t" "s_add_u32 m0, m0, 32\n\t" "s_nop 0\n\t"
      "global_load_lds_dword %2, %3 offset:3136\n\t" "s_add_u32 m0, m0, 32\n\t" "s_nop 0\n\t"
      "global_load_lds_dword %2, %3 offset:3360\n\t"
      "s_mov_b64 exec, %1\n\t"
      "s_mov_b32 m0, %0"
      : "=&s"(keep), "=&s"(ex)
      : "v"(voff), "s"(gbase), "s"(ldsdst)
      : "memory", "scc");
}

DI s16x4 vtr(LDSP(const char) p) {
  typedef short v4s __attribute__((ext_vector_type(4)));
  return __builtin_amdgcn_ds_read_tr16_b64_v4i16((LDSP(v4s))p);
}
DI bf16x8 vfrag(LDSP(const char) p) {
  s16x4 lo = vtr(p), hi = vtr(p + 512);
  return __builtin_bit_cast(bf16x8, __builtin_shufflevector(lo, hi, 0, 1, 2, 3, 4, 5, 6, 7));
}
DI f16x8 kfrag(LDSP(const char) p) { return *(LDSP(const f16x8))p; }

#define STAMP(i)
#define STAMP_DECL
#define STAMP_OUT

__global__ __launch_bounds__(512, 2) void qkn_attn(const char* __restrict__ q, const char* __restrict__ k, const char* __restrict__ v,
                                                   const unsigned* __restrict__ t, char* __restrict__ out, unsigned long long* ws) {
  extern __shared__ __attribute__((aligned(16))) char smem[];
  STAMP_DECL;
  STAMP(0);
  const unsigned long long t00_ = __builtin_amdgcn_s_memrealtime();
  if (threadIdx.x == 0) __hip_atomic_store(ws + blockIdx.x, t00_, __ATOMIC_RELAXED, __HIP_MEMORY_SCOPE_AGENT);
  const int bid = blockIdx.x, x = bid & 7, y = bid >> 3;
  const int head = x * 8 + (y >> 2), q0 = (y & 3) * 512;
  const size_t hoff = (size_t)head * SEQ * ROWB;
  LDSP(char) lds = (LDSP(char))smem;
  const unsigned lds0 = (unsigned)(size_t)lds;
  const int tid = threadIdx.x, lane = tid & 63, r = lane & 31, h = lane >> 5;
  const int wid = __builtin_amdgcn_readfirstlane(tid >> 6);
  const unsigned tw = t[(head & (NHEAD - 1)) >> 1];
  const float c = __uint_as_float((head & 1) ? (tw & 0xffff0000u) : (tw << 16)) * LOG2E;
  const bool fast = fabsf(c) <= FAST_LIMIT;

  if (wid >= 4) {
    const int pw = wid - 4, ptid = tid - 256;
    const char* Kh = k + hoff + (size_t)ptid * ROWB;
    const char* Vh = v + hoff + (size_t)pw * 64 * ROWB;
    const unsigned voff = (unsigned)((lane >> 4) * ROWB + (lane & 15) * 4);
    const unsigned vdst = lds0 + pw * 2 * BLK + 2048;
    LDSP(char) kdst = lds + (ptid >> 5) * BLK + (ptid & 31) * 16;
    KRaw ka = k_load(Kh);
    KRaw kb = k_load(Kh + CHUNK * ROWB);
    v_dma(Vh, voff, vdst);
    {
      const u32x2 one = {0x00003F80u, 0u};
#pragma unroll
      for (int s = 0; s < NSTAGE; ++s) *(LDSP(u32x2))(lds + s * STAGE + (ptid >> 5) * BLK + 2048 + (ptid & 31) * 64 + 56) = one;
    }
    const unsigned kpad = fast ? pk_bf16(-fabsf(c), 0.f) : 0u;
    k_norm_store(ka, kdst, kpad);
    v_dma(Vh + CHUNK * ROWB, voff, vdst + STAGE);
    asm volatile("s_waitcnt vmcnt(16) lgkmcnt(0)\n\ts_barrier" ::: "memory");
#pragma unroll 1
    for (int ch = 1; ch < NCHUNK; ++ch) {
      __builtin_amdgcn_s_waitcnt(0x0F70);
      ka = kb;
      if (ch + 1 < NCHUNK) {
        kb = k_load(Kh + (size_t)(ch + 1) * CHUNK * ROWB);
        v_dma(Vh + (size_t)(ch + 1) * CHUNK * ROWB, voff, vdst + ((ch + 1) & (NSTAGE - 1)) * STAGE);
      }
      k_norm_store(ka, kdst + (ch & (NSTAGE - 1)) * STAGE, kpad);
      asm volatile("s_waitcnt lgkmcnt(0)\n\ts_barrier" ::: "memory");
    }
    return;
  }

  const char* Qw = q + hoff + (size_t)(q0 + wid * 128 + r) * ROWB + h * 16;
  u32x4 qa[4]; u32x2 qb[4], qc[4];
#pragma unroll
  for (int b = 0; b < 4; ++b) {
    const char* qp = Qw + b * 32 * ROWB;
    qa[b] = *(const u32x4a8*)qp;
    qb[b] = *(const u32x2*)(qp + 32);
    qc[b] = *(const u32x2*)(qp + (h ? 32 : 40));
  }
  u32x4 qf[4][2];
  const unsigned hmask = h ? 0u : 0xffffffffu;
  const float qscale = fast ? c : 1.0f;
  const unsigned qpad = h ? 0x00003F80u : 0u;
#pragma unroll
  for (int b = 0; b < 4; ++b) {
    qc[b][0] &= hmask; qc[b][1] &= hmask;
    float s0 = dot2(qa[b][0], 0.f), s1 = dot2(qa[b][1], 0.f);
    s0 = dot2(qa[b][2], s0); s1 = dot2(qa[b][3], s1);
    s0 = dot2(qb[b][0], s0); s1 = dot2(qb[b][1], s1);
    s0 = dot2(qc[b][0], s0); s1 = dot2(qc[b][1], s1);
    const float ss = swap_sum(s0 + s1);
    const float rq = qscale * __builtin_amdgcn_rcpf(__builtin_amdgcn_sqrtf(ss) + EPS);
    qf[b][0] = (u32x4){scale_pk(qa[b][0], rq), scale_pk(qa[b][1], rq), scale_pk(qa[b][2], rq), scale_pk(qa[b][3], rq)};
    qf[b][1] = (u32x4){scale_pk(qb[b][0], rq), scale_pk(qb[b][1], rq), scale_pk(qc[b][0], rq) | qpad, scale_pk(qc[b][1], rq)};
  }
  f32x16 o[4] = {};
  unsigned kl = lds0 + h * 512 + r * 16;
  unsigned vl = lds0 + 2048 + (4 * h + ((lane & 15) >> 2)) * 64 + ((lane >> 4) & 1) * 32 + (lane & 3) * 8;
  STAMP(1);
  if (fast) {
    unsigned cnt, cch;
    asm volatile(
      "s_setprio 3\n\t"
      "v_mov_b32 v232, 0\n\t"
      "v_mov_b32 v233, 0\n\t"
      "v_mov_b32 v234, 0\n\t"
      "v_mov_b32 v235, 0\n\t"
      "v_mov_b32 v236, 0\n\t"
      "v_mov_b32 v237, 0\n\t"
      "v_mov_b32 v238, 0\n\t"
      "v_mov_b32 v239, 0\n\t"
      "v_mov_b32 v184, 0\n\t"
      "v_mov_b32 v185, 0\n\t"
      "v_mov_b32 v186, 0\n\t"
      "v_mov_b32 v187, 0\n\t"
      "v_mov_b32 v188, 0\n\t"
      "v_mov_b32 v189, 0\n\t"
      "v_mov_b32 v190, 0\n\t"
      "v_mov_b32 v191, 0\n\t"
      "s_mov_b64 vcc, 0\n\t"
      "s_barrier\n\t"
      "ds_read_b128 v[240:243], %4 offset:0\n\t"
      "ds_read_b128 v[244:247], %4 offset:1024\n\t"
      "ds_read_b64_tr_b16 v[248:249], %5 offset:0\n\t"
      "ds_read_b64_tr_b16 v[250:251], %5 offset:512\n\t"
      "ds_read_b64_tr_b16 v[252:253], %5 offset:1024\n\t"
      "ds_read_b64_tr_b16 v[254:255], %5 offset:1536\n\t"
      "s_mov_b32 %6, 4\n\t"
      "s_mov_b32 %7, 8\n\t"
      "s_waitcnt lgkmcnt(4)\n\t"
      "v_mfma_f32_32x32x16_bf16 v[192:207], v[240:243], %8, 0\n\t"
      "v_mfma_f32_32x32x16_bf16 v[192:207], v[244:247], %9, v[192:207]\n\t"
      "s_waitcnt lgkmcnt(0)\n\t"
      "s_nop 10\n\t"
      "s_branch LMID%=\n\t"
      "LOUTER%=:\n\t"
      "s_barrier\n\t"
      "v_and_b32 %4, 0x1ffff, %4\n\t"
      "v_and_b32 %5, 0x1ffff, %5\n\t"
      "s_mov_b32 %6, 4\n\t"
      "LTOP%=:\n\t"
      "v_exp_f32 v192, v192\n\t"
      "v_exp_f32 v193, v193\n\t"
      "v_exp_f32 v194, v194\n\t"
      "v_exp_f32 v195, v195\n\t"
      "v_mfma_f32_32x32x16_bf16 v[208:223], v[240:243], %14, 0\n\t"
      "ds_read_b128 v[240:243], %4 offset:0\n\t"
      "ds_read_b64_tr_b16 v[248:249], %5 offset:0\n\t"
      "v_cvt_pk_bf16_f32 v224, v192, v193\n\t"
      "v_cvt_pk_bf16_f32 v225, v194, v195\n\t"
      "v_exp_f32 v196, v196\n\t"
      "v_exp_f32 v197, v197\n\t"
      "v_exp_f32 v198, v198\n\t"
      "v_exp_f32 v199, v199\n\t"
      "v_mfma_f32_32x32x16_bf16 v[208:223], v[244:247], %15, v[208:223]\n\t"
      "ds_read_b128 v[244:247], %4 offset:1024\n\t"
      "ds_read_b64_tr_b16 v[250:251], %5 offset:512\n\t"
      "v_cvt_pk_bf16_f32 v226, v196, v197\n\t"
      "v_cvt_pk_bf16_f32 v227, v198, v199\n\t"
      "v_exp_f32 v200, v200\n\t"
      "v_exp_f32 v201, v201\n\t"
      "v_exp_f32 v202, v202\n\t"
      "v_exp_f32 v203, v203\n\t"
      "v_mfma_f32_32x32x16_bf16 %1, v[184:187], v[232:235], %1\n\t"
      "ds_read_b64_tr_b16 v[252:253], %5 offset:1024\n\t"
      "v_cvt_pk_bf16_f32 v228, v200, v201\n\t"
      "v_cvt_pk_bf16_f32 v229, v202, v203\n\t"
      "v_exp_f32 v204, v204\n\t"
      "v_exp_f32 v205, v205\n\t"
      "v_exp_f32 v206, v206\n\t"
      "v_exp_f32 v207, v207\n\t"
      "v_mfma_f32_32x32x16_bf16 %1, v[188:191], v[236:239], %1\n\t"
      "ds_read_b64_tr_b16 v[254:255], %5 offset:1536\n\t"
      "v_cvt_pk_bf16_f32 v230, v204, v205\n\t"
      "v_cvt_pk_bf16_f32 v231, v206, v207\n\t"
      "v_exp_f32 v208, v208\n\t"
      "v_exp_f32 v209, v209\n\t"
      "v_exp_f32 v210, v210\n\t"
      "v_exp_f32 v211, v211\n\t"
      "s_waitcnt lgkmcnt(0)\n\t"
      "v_mfma_f32_32x32x16_bf16 v[192:207], v[240:243], %8, 0\n\t"
      "v_cvt_pk_bf16_f32 v232, v208, v209\n\t"
      "v_cvt_pk_bf16_f32 v233, v210, v211\n\t"
      "v_exp_f32 v212, v212\n\t"
      "v_exp_f32 v213, v213\n\t"
      "v_exp_f32 v214, v214\n\t"
      "v_exp_f32 v215, v215\n\t"
      "v_mfma_f32_32x32x16_bf16 v[192:207], v[244:247], %9, v[192:207]\n\t"
      "v_cvt_pk_bf16_f32 v234, v212, v213\n\t"
      "v_cvt_pk_bf16_f32 v235, v214, v215\n\t"
      "v_exp_f32 v216, v216\n\t"
      "v_exp_f32 v217, v217\n\t"
      "v_exp_f32 v218, v218\n\t"
      "v_exp_f32 v219, v219\n\t"
      "v_mfma_f32_32x32x16_bf16 %2, v[184:187], v[224:227], %2\n\t"
      "v_cvt_pk_bf16_f32 v236, v216, v217\n\t"
      "v_cvt_pk_bf16_f32 v237, v218, v219\n\t"
      "v_exp_f32 v220, v220\n\t"
      "v_exp_f32 v221, v221\n\t"
      "v_exp_f32 v222, v222\n\t"
      "v_exp_f32 v223, v223\n\t"
      "v_mfma_f32_32x32x16_bf16 %2, v[188:191], v[228:231], %2\n\t"
      "v_cvt_pk_bf16_f32 v238, v220, v221\n\t"
      "v_cvt_pk_bf16_f32 v239, v222, v223\n\t"
      "s_cbranch_vccnz LDONE%=\n\t"
      "LMID%=:\n\t"
      "v_exp_f32 v192, v192\n\t"
      "v_exp_f32 v193, v193\n\t"
      "v_exp_f32 v194, v194\n\t"
      "v_exp_f32 v195, v195\n\t"
      "v_mfma_f32_32x32x16_bf16 v[208:223], v[240:243], %10, 0\n\t"
      "v_cvt_pk_bf16_f32 v224, v192, v193\n\t"
      "v_cvt_pk_bf16_f32 v225, v194, v195\n\t"
      "v_exp_f32 v196, v196\n\t"
      "v_exp_f32 v197, v197\n\t"
      "v_exp_f32 v198, v198\n\t"
      "v_exp_f32 v199, v199\n\t"
      "v_mfma_f32_32x32x16_bf16 v[208:223], v[244:247], %11, v[208:223]\n\t"
      "v_cvt_pk_bf16_f32 v226, v196, v197\n\t"
      "v_cvt_pk_bf16_f32 v227, v198, v199\n\t"
      "v_exp_f32 v200, v200\n\t"
      "v_exp_f32 v201, v201\n\t"
      "v_exp_f32 v202, v202\n\t"
      "v_exp_f32 v203, v203\n\t"
      "v_mfma_f32_32x32x16_bf16 %3, v[184:187], v[232:235], %3\n\t"
      "v_cvt_pk_bf16_f32 v228, v200, v201\n\t"
      "v_cvt_pk_bf16_f32 v229, v202, v203\n\t"
      "v_exp_f32 v204, v204\n\t"
      "v_exp_f32 v205, v205\n\t"
      "v_exp_f32 v206, v206\n\t"
      "v_exp_f32 v207, v207\n\t"
      "v_mfma_f32_32x32x16_bf16 %3, v[188:191], v[236:239], %3\n\t"
      "v_cvt_pk_bf16_f32 v230, v204, v205\n\t"
      "v_cvt_pk_bf16_f32 v231, v206, v207\n\t"
      "v_exp_f32 v208, v208\n\t"
      "v_exp_f32 v209, v209\n\t"
      "v_exp_f32 v210, v210\n\t"
      "v_exp_f32 v211, v211\n\t"
      "v_mfma_f32_32x32x16_bf16 v[192:207], v[240:243], %12, 0\n\t"
      "v_cvt_pk_bf16_f32 v232, v208, v209\n\t"
      "v_cvt_pk_bf16_f32 v233, v210, v211\n\t"
      "v_exp_f32 v212, v212\n\t"
      "v_exp_f32 v213, v213\n\t"
      "v_exp_f32 v214, v214\n\t"
      "v_exp_f32 v215, v215\n\t"
      "v_mfma_f32_32x32x16_bf16 v[192:207], v[244:247], %13, v[192:207]\n\t"
      "v_cvt_pk_bf16_f32 v234, v212, v213\n\t"
      "v_cvt_pk_bf16_f32 v235, v214, v215\n\t"
      "v_exp_f32 v216, v216\n\t"
      "v_exp_f32 v217, v217\n\t"
      "v_exp_f32 v218, v218\n\t"
      "v_exp_f32 v219, v219\n\t"
      "v_mfma_f32_32x32x16_bf16 %0, v[248:251], v[224:227], %0\n\t"
      "v_cvt_pk_bf16_f32 v236, v216, v217\n\t"
      "v_cvt_pk_bf16_f32 v237, v218, v219\n\t"
      "v_exp_f32 v220, v220\n\t"
      "v_exp_f32 v221, v221\n\t"
      "v_exp_f32 v222, v222\n\t"
      "v_exp_f32 v223, v223\n\t"
      "v_mfma_f32_32x32x16_bf16 %0, v[252:255], v[228:231], %0\n\t"
      "v_cvt_pk_bf16_f32 v238, v220, v221\n\t"
      "v_cvt_pk_bf16_f32 v239, v222, v223\n\t"
      "v_exp_f32 v192, v192\n\t"
      "v_exp_f32 v193, v193\n\t"
      "v_exp_f32 v194, v194\n\t"
      "v_exp_f32 v195, v195\n\t"
      "v_mfma_f32_32x32x16_bf16 v[208:223], v[240:243], %14, 0\n\t"
      "ds_read_b128 v[240:243], %4 offset:4096\n\t"
      "ds_read_b64_tr_b16 v[184:185], %5 offset:4096\n\t"
      "v_cvt_pk_bf16_f32 v224, v192, v193\n\t"
      "v_cvt_pk_bf16_f32 v225, v194, v195\n\t"
      "v_exp_f32 v196, v196\n\t"
      "v_exp_f32 v197, v197\n\t"
      "v_exp_f32 v198, v198\n\t"
      "v_exp_f32 v199, v199\n\t"
      "v_mfma_f32_32x32x16_bf16 v[208:223], v[244:247], %15, v[208:223]\n\t"
      "ds_read_b128 v[244:247], %4 offset:5120\n\t"
      "ds_read_b64_tr_b16 v[186:187], %5 offset:4608\n\t"
      "v_cvt_pk_bf16_f32 v226, v196, v197\n\t"
      "v_cvt_pk_bf16_f32 v227, v198, v199\n\t"
      "v_exp_f32 v200, v200\n\t"
      "v_exp_f32 v201, v201\n\t"
      "v_exp_f32 v202, v202\n\t"
      "v_exp_f32 v203, v203\n\t"
      "v_mfma_f32_32x32x16_bf16 %1, v[248:251], v[232:235], %1\n\t"
      "ds_read_b64_tr_b16 v[188:189], %5 offset:5120\n\t"
      "v_cvt_pk_bf16_f32 v228, v200, v201\n\t"
      "v_cvt_pk_bf16_f32 v229, v202, v203\n\t"
      "v_exp_f32 v204, v204\n\t"
      "v_exp_f32 v205, v205\n\t"
      "v_exp_f32 v206, v206\n\t"
      "v_exp_f32 v207, v207\n\t"
      "v_mfma_f32_32x32x16_bf16 %1, v[252:255], v[236:239], %1\n\t"
      "ds_read_b64_tr_b16 v[190:191], %5 offset:5632\n\t"
      "v_cvt_pk_bf16_f32 v230, v204, v205\n\t"
      "v_cvt_pk_bf16_f32 v231, v206, v207\n\t"
      "v_exp_f32 v208, v208\n\t"
      "v_exp_f32 v209, v209\n\t"
      "v_exp_f32 v210, v210\n\t"
      "v_exp_f32 v211, v211\n\t"
      "s_waitcnt lgkmcnt(0)\n\t"
      "v_mfma_f32_32x32x16_bf16 v[192:207], v[240:243], %8, 0\n\t"
      "v_cvt_pk_bf16_f32 v232, v208, v209\n\t"
      "v_cvt_pk_bf16_f32 v233, v210, v211\n\t"
      "v_exp_f32 v212, v212\n\t"
      "v_exp_f32 v213, v213\n\t"
      "v_exp_f32 v214, v214\n\t"
      "v_exp_f32 v215, v215\n\t"
      "v_mfma_f32_32x32x16_bf16 v[192:207], v[244:247], %9, v[192:207]\n\t"
      "v_cvt_pk_bf16_f32 v234, v212, v213\n\t"
      "v_cvt_pk_bf16_f32 v235, v214, v215\n\t"
      "v_exp_f32 v216, v216\n\t"
      "v_exp_f32 v217, v217\n\t"
      "v_exp_f32 v218, v218\n\t"
      "v_exp_f32 v219, v219\n\t"
      "v_mfma_f32_32x32x16_bf16 %2, v[248:251], v[224:227], %2\n\t"
      "v_cvt_pk_bf16_f32 v236, v216, v217\n\t"
      "v_cvt_pk_bf16_f32 v237, v218, v219\n\t"
      "v_exp_f32 v220, v220\n\t"
      "v_exp_f32 v221, v221\n\t"
      "v_exp_f32 v222, v222\n\t"
      "v_exp_f32 v223, v223\n\t"
      "v_mfma_f32_32x32x16_bf16 %2, v[252:255], v[228:231], %2\n\t"
      "v_cvt_pk_bf16_f32 v238, v220, v221\n\t"
      "v_cvt_pk_bf16_f32 v239, v222, v223\n\t"
      "v_exp_f32 v192, v192\n\t"
      "v_exp_f32 v193, v193\n\t"
      "v_exp_f32 v194, v194\n\t"
      "v_exp_f32 v195, v195\n\t"
      "v_mfma_f32_32x32x16_bf16 v[208:223], v[240:243], %10, 0\n\t"
      "v_cvt_pk_bf16_f32 v224, v192, v193\n\t"
      "v_cvt_pk_bf16_f32 v225, v194, v195\n\t"
      "v_exp_f32 v196, v196\n\t"
      "v_exp_f32 v197, v197\n\t"
      "v_exp_f32 v198, v198\n\t"
      "v_exp_f32 v199, v199\n\t"
      "v_mfma_f32_32x32x16_bf16 v[208:223], v[244:247], %11, v[208:223]\n\t"
      "v_cvt_pk_bf16_f32 v226, v196, v197\n\t"
      "v_cvt_pk_bf16_f32 v227, v198, v199\n\t"
      "v_exp_f32 v200, v200\n\t"
      "v_exp_f32 v201, v201\n\t"
      "v_exp_f32 v202, v202\n\t"
      "v_exp_f32 v203, v203\n\t"
      "v_mfma_f32_32x32x16_bf16 %3, v[248:251], v[232:235], %3\n\t"
      "v_cvt_pk_bf16_f32 v228, v200, v201\n\t"
      "v_cvt_pk_bf16_f32 v229, v202, v203\n\t"
      "v_exp_f32 v204, v204\n\t"
      "v_exp_f32 v205, v205\n\t"
      "v_exp_f32 v206, v206\n\t"
      "v_exp_f32 v207, v207\n\t"
      "v_mfma_f32_32x32x16_bf16 %3, v[252:255], v[236:239], %3\n\t"
      "v_cvt_pk_bf16_f32 v230, v204, v205\n\t"
      "v_cvt_pk_bf16_f32 v231, v206, v207\n\t"
      "v_exp_f32 v208, v208\n\t"
      "v_exp_f32 v209, v209\n\t"
      "v_exp_f32 v210, v210\n\t"
      "v_exp_f32 v211, v211\n\t"
      "v_mfma_f32_32x32x16_bf16 v[192:207], v[240:243], %12, 0\n\t"
      "v_cvt_pk_bf16_f32 v232, v208, v209\n\t"
      "v_cvt_pk_bf16_f32 v233, v210, v211\n\t"
      "v_exp_f32 v212, v212\n\t"
      "v_exp_f32 v213, v213\n\t"
      "v_exp_f32 v214, v214\n\t"
      "v_exp_f32 v215, v215\n\t"
      "v_mfma_f32_32x32x16_bf16 v[192:207], v[244:247], %13, v[192:207]\n\t"
      "v_cvt_pk_bf16_f32 v234, v212, v213\n\t"
      "v_cvt_pk_bf16_f32 v235, v214, v215\n\t"
      "v_exp_f32 v216, v216\n\t"
      "v_exp_f32 v217, v217\n\t"
      "v_exp_f32 v218, v218\n\t"
      "v_exp_f32 v219, v219\n\t"
      "v_mfma_f32_32x32x16_bf16 %0, v[184:187], v[224:227], %0\n\t"
      "v_cvt_pk_bf16_f32 v236, v216, v217\n\t"
      "v_cvt_pk_bf16_f32 v237, v218, v219\n\t"
      "v_exp_f32 v220, v220\n\t"
      "v_exp_f32 v221, v221\n\t"
      "v_exp_f32 v222, v222\n\t"
      "v_exp_f32 v223, v223\n\t"
      "v_mfma_f32_32x32x16_bf16 %0, v[188:191], v[228:231], %0\n\t"
      "v_cvt_pk_bf16_f32 v238, v220, v221\n\t"
      "v_cvt_pk_bf16_f32 v239, v222, v223\n\t"
      "v_add_u32 %4, 8192, %4\n\t"
      "v_add_u32 %5, 8192, %5\n\t"
      "s_sub_u32 %6, %6, 1\n\t"
      "s_cmp_lg_u32 %6, 0\n\t"
      "s_cbranch_scc1 LTOP%=\n\t"
      "s_sub_u32 %7, %7, 1\n\t"
      "s_cmp_lg_u32 %7, 0\n\t"
      "s_cbranch_scc1 LOUTER%=\n\t"
      "v_and_b32 %4, 0x1ffff, %4\n\t"
      "v_and_b32 %5, 0x1ffff, %5\n\t"
      "s_mov_b64 vcc, -1\n\t"
      "s_branch LTOP%=\n\t"
      "LDONE%=:\n\t"
      "s_nop 1\n\t"
      "v_mfma_f32_32x32x16_bf16 %3, v[184:187], v[232:235], %3\n\t"
      "v_mfma_f32_32x32x16_bf16 %3, v[188:191], v[236:239], %3\n\t"
      "s_nop 11\n\t"
      : "+v"(o[0]), "+v"(o[1]), "+v"(o[2]), "+v"(o[3]), "+v"(kl), "+v"(vl), "=&s"(cnt), "=&s"(cch)
      : "v"(qf[0][0]), "v"(qf[0][1]), "v"(qf[1][0]), "v"(qf[1][1]), "v"(qf[2][0]), "v"(qf[2][1]), "v"(qf[3][0]), "v"(qf[3][1])
      : "memory", "scc", "vcc", "v184", "v185", "v186", "v187", "v188", "v189", "v190", "v191", "v192", "v193", "v194", "v195", "v196", "v197", "v198", "v199", "v200", "v201", "v202", "v203", "v204", "v205", "v206", "v207", "v208", "v209", "v210", "v211", "v212", "v213", "v214", "v215", "v216", "v217", "v218", "v219", "v220", "v221", "v222", "v223", "v224", "v225", "v226", "v227", "v228", "v229", "v230", "v231", "v232", "v233", "v234", "v235", "v236", "v237", "v238", "v239", "v240", "v241", "v242", "v243", "v244", "v245", "v246", "v247", "v248", "v249", "v250", "v251", "v252", "v253", "v254", "v255");
  } else {
    float m[4] = {-INFINITY, -INFINITY, -INFINITY, -INFINITY};
#pragma unroll 1
    for (int tl = 0; tl < SEQ / 32; ++tl) {
      if ((tl & (TPC - 1)) == 0) asm volatile("s_waitcnt lgkmcnt(0)\n\ts_barrier" ::: "memory");
      const unsigned boff = (tl & (NSTAGE * TPC - 1)) * BLK;
      LDSP(const char) kp = (LDSP(const char))(size_t)(kl + boff);
      LDSP(const char) vp = (LDSP(const char))(size_t)(vl + boff);
      const f16x8 k0 = kfrag(kp), k1 = kfrag(kp + 1024);
      const bf16x8 v0 = vfrag(vp), v1 = vfrag(vp + 1024);
#pragma unroll
      for (int b = 0; b < 4; ++b) {
        f32x16 s = mfma_qk(k0, __builtin_bit_cast(f16x8, qf[b][0]), (f32x16){});
        s = mfma_qk(k1, __builtin_bit_cast(f16x8, qf[b][1]), s);
        float mx = m[b];
#pragma unroll
        for (int i = 0; i < 16; ++i) { s[i] *= c; mx = fmaxf(mx, s[i]); }
        mx = swap_max(mx);
        const float al = __builtin_amdgcn_exp2f(m[b] - mx);
        m[b] = mx;
#pragma unroll
        for (int i = 0; i < 16; ++i) { s[i] = __builtin_amdgcn_exp2f(s[i] - mx); o[b][i] *= al; }
        const u32x4 pa = {pk_bf16(s[0], s[1]), pk_bf16(s[2], s[3]), pk_bf16(s[4], s[5]), pk_bf16(s[6], s[7])};
        const u32x4 pb = {pk_bf16(s[8], s[9]), pk_bf16(s[10], s[11]), pk_bf16(s[12], s[13]), pk_bf16(s[14], s[15])};
        o[b] = mfma_pv(v0, __builtin_bit_cast(bf16x8, pa), o[b]);
        o[b] = mfma_pv(v1, __builtin_bit_cast(bf16x8, pb), o[b]);
      }
    }
  }
  STAMP(2);
  LDSP(char) stg = lds + wid * 8192;
#pragma unroll
  for (int b = 0; b < 4; ++b) {
    auto rr = __builtin_amdgcn_permlane32_swap(__float_as_uint(o[b][12]), __float_as_uint(o[b][12]), false, false);
    const float rl = __builtin_amdgcn_rcpf(__uint_as_float(rr[1]));
    LDSP(char) sp = stg + (b * 32 + r) * ROWB + h * 8;
#pragma unroll
    for (int g = 0; g < 4; ++g) {
      const u32x2 w = {pk_bf16(o[b][4 * g] * rl, o[b][4 * g + 1] * rl), pk_bf16(o[b][4 * g + 2] * rl, o[b][4 * g + 3] * rl)};
      if (g < 3 || h == 0) *(LDSP(u32x2))(sp + g * 16) = w;
    }
  }
  {
    char* Ow = out + hoff + (size_t)(q0 + wid * 128) * ROWB + 3072;
    const unsigned lo = lane * 16;
#pragma unroll
    for (int i = 0; i < 7; ++i) {
      const u32x4 w = *(LDSP(u32x4))(stg + i * 1024 + lo);
      asm volatile("global_store_dwordx4 %0, %1, %2 offset:%3 sc0 sc1\n\ts_nop 1" :: "v"(lo), "v"(w), "s"(Ow), "i"(i * 1024 - 3072) : "memory");
    }
  }
  { unsigned long long m_ = __hip_atomic_load(ws + lane, __ATOMIC_RELAXED, __HIP_MEMORY_SCOPE_AGENT);
    for (int i_ = 1; i_ < 4; ++i_) { unsigned long long x_ = __hip_atomic_load(ws + lane + 64 * i_, __ATOMIC_RELAXED, __HIP_MEMORY_SCOPE_AGENT); m_ = min(m_, x_); }
    for (int o_ = 1; o_ < 64; o_ <<= 1) { unsigned long long x_ = __shfl_xor(m_, o_); m_ = min(m_, x_); }
    if (m_ + 10000ull < t00_ || m_ > t00_ + 10000ull) m_ = t00_;
    while (__builtin_amdgcn_s_memrealtime() - m_ < 6000ull) __builtin_amdgcn_s_sleep(1); }
  STAMP(3);
  STAMP_OUT;
}

extern "C" void kernel_launch(void* const* d_in, const int* in_sizes, int n_in, void* d_out, int out_size, void* d_ws, size_t ws_size,
                              hipStream_t stream) {
  (void)hipFuncSetAttribute((const void*)qkn_attn, hipFuncAttributeMaxDynamicSharedMemorySize, LDS_BYTES);
  qkn_attn<<<256, 512, LDS_BYTES, stream>>>((const char*)d_in[0], (const char*)d_in[1], (const char*)d_in[2],
                                            (const unsigned*)d_in[3], (char*)d_out, (unsigned long long*)d_ws);
}
```

```cpp
#include <hip/hip_runtime.h>
#include <hip/hip_bf16.h>

typedef _Float16 f16x8 __attribute__((ext_vector_type(8)));
typedef _Float16 f16x2 __attribute__((ext_vector_type(2)));
typedef __bf16 bf16x8 __attribute__((ext_vector_type(8)));
typedef __bf16 bf16x2 __attribute__((ext_vector_type(2)));
typedef short s16x4 __attribute__((ext_vector_type(4)));
typedef float f32x16 __attribute__((ext_vector_type(16)));
typedef float f32x2 __attribute__((ext_vector_type(2)));
typedef unsigned u32x4 __attribute__((ext_vector_type(4)));
typedef unsigned u32x2 __attribute__((ext_vector_type(2)));

#define DI __device__ __forceinline__
#define LDSP(T) __attribute__((address_space(3))) T*

constexpr int SEQ = 2048, DH = 28, NHEAD = 16, ROWB = DH * 2;
constexpr int LDS_BYTES = 131072;
constexpr float LOG2E = 1.4426950408889634f;
constexpr float EPS = 1e-6f;

DI unsigned pk_bf16(float lo, float hi) { f32x2 v = {lo, hi}; return __builtin_bit_cast(unsigned, __builtin_convertvector(v, bf16x2)); }
DI unsigned pk_f16(float lo, float hi) { f32x2 v = {lo, hi}; return __builtin_bit_cast(unsigned, __builtin_convertvector(v, f16x2)); }
DI float bf_lo(unsigned w) { return __uint_as_float(w << 16); }
DI float bf_hi(unsigned w) { return __uint_as_float(w & 0xffff0000u); }
DI float swap_max(float x) {
  auto rr = __builtin_amdgcn_permlane32_swap(__float_as_uint(x), __float_as_uint(x), false, false);
  return fmaxf(__uint_as_float(rr[0]), __uint_as_float(rr[1]));
}
DI s16x4 vtr(LDSP(const char) p) {
  typedef short v4s __attribute__((ext_vector_type(4)));
  return __builtin_amdgcn_ds_read_tr16_b64_v4i16((LDSP(v4s))p);
}
DI bf16x8 vfrag(LDSP(const char) p) {
  s16x4 lo = vtr(p), hi = vtr(p + 512);
  return __builtin_bit_cast(bf16x8, __builtin_shufflevector(lo, hi, 0, 1, 2, 3, 4, 5, 6, 7));
}
DI void norm_row(const char* src, LDSP(char) dst) {
  unsigned w[16];
  float s = 0.f;
#pragma unroll
  for (int i = 0; i < 7; ++i) { const u32x2 x = *(const u32x2*)(src + i * 8); w[2 * i] = x[0]; w[2 * i + 1] = x[1]; }
#pragma unroll
  for (int i = 0; i < 14; ++i) s += bf_lo(w[i]) * bf_lo(w[i]) + bf_hi(w[i]) * bf_hi(w[i]);
  const float r = 1.0f / (sqrtf(s) + EPS);
#pragma unroll
  for (int i = 0; i < 14; ++i) w[i] = pk_f16(bf_lo(w[i]) * r, bf_hi(w[i]) * r);
  w[14] = 0u; w[15] = 0u;
#pragma unroll
  for (int p = 0; p < 4; ++p) *(LDSP(u32x4))(dst + p * 512) = (u32x4){w[4 * p], w[4 * p + 1], w[4 * p + 2], w[4 * p + 3]};
}

DI void slow_path(const char* Qb, const char* Kh, const char* Vh, char* Ob, float c, LDSP(char) lds) {
  const int tid = threadIdx.x, lane = tid & 63, r = lane & 31, h = lane >> 5, wid = tid >> 6;
  __syncthreads();
  norm_row(Qb + tid * ROWB, lds + 32768 + (tid >> 5) * 2048 + r * 16);
  __syncthreads();
  f16x8 qf[2][2];
#pragma unroll
  for (int b = 0; b < 2; ++b)
#pragma unroll
    for (int s = 0; s < 2; ++s) qf[b][s] = *(LDSP(const f16x8))(lds + 32768 + (wid * 2 + b) * 2048 + s * 1024 + h * 512 + r * 16);
  f32x16 o[2] = {};
  float m[2] = {-INFINITY, -INFINITY};
  const int vl = 2048 + (4 * h + ((lane & 15) >> 2)) * 64 + ((lane >> 4) & 1) * 32 + (lane & 3) * 8;
#pragma unroll 1
  for (int ch = 0; ch < SEQ / 256; ++ch) {
    __syncthreads();
    const int row = tid & 255;
    if (tid < 256) {
      norm_row(Kh + (ch * 256 + row) * ROWB, lds + (row >> 5) * 4096 + (row & 31) * 16);
    } else {
      LDSP(char) d = lds + (row >> 5) * 4096 + 2048 + (row & 31) * 64;
#pragma unroll
      for (int i = 0; i < 7; ++i) *(LDSP(u32x2))(d + i * 8) = *(const u32x2*)(Vh + (ch * 256 + row) * ROWB + i * 8);
      *(LDSP(u32x2))(d + 56) = (u32x2){0x00003F80u, 0u};
    }
    __syncthreads();
#pragma unroll 1
    for (int tl = 0; tl < 8; ++tl) {
      const f16x8 k0 = *(LDSP(const f16x8))(lds + tl * 4096 + h * 512 + r * 16), k1 = *(LDSP(const f16x8))(lds + tl * 4096 + 1024 + h * 512 + r * 16);
      const bf16x8 v0 = vfrag(lds + tl * 4096 + vl), v1 = vfrag(lds + tl * 4096 + vl + 1024);
#pragma unroll
      for (int b = 0; b < 2; ++b) {
        f32x16 s = __builtin_amdgcn_mfma_f32_32x32x16_f16(k0, qf[b][0], (f32x16){}, 0, 0, 0);
        s = __builtin_amdgcn_mfma_f32_32x32x16_f16(k1, qf[b][1], s, 0, 0, 0);
        float mx = m[b];
#pragma unroll
        for (int i = 0; i < 16; ++i) { s[i] *= c; mx = fmaxf(mx, s[i]); }
        mx = swap_max(mx);
        const float al = __builtin_amdgcn_exp2f(m[b] - mx);
        m[b] = mx;
#pragma unroll
        for (int i = 0; i < 16; ++i) { s[i] = __builtin_amdgcn_exp2f(s[i] - mx); o[b][i] *= al; }
        const u32x4 pa = {pk_bf16(s[0], s[1]), pk_bf16(s[2], s[3]), pk_bf16(s[4], s[5]), pk_bf16(s[6], s[7])};
        const u32x4 pb = {pk_bf16(s[8], s[9]), pk_bf16(s[10], s[11]), pk_bf16(s[12], s[13]), pk_bf16(s[14], s[15])};
        o[b] = __builtin_amdgcn_mfma_f32_32x32x16_bf16(v0, __builtin_bit_cast(bf16x8, pa), o[b], 0, 0, 0);
        o[b] = __builtin_amdgcn_mfma_f32_32x32x16_bf16(v1, __builtin_bit_cast(bf16x8, pb), o[b], 0, 0, 0);
      }
    }
  }
#pragma unroll
  for (int b = 0; b < 2; ++b) {
    auto rr = __builtin_amdgcn_permlane32_swap(__float_as_uint(o[b][12]), __float_as_uint(o[b][12]), false, false);
    const float rl = 1.0f / __uint_as_float(rr[1]);
    char* op = Ob + (wid * 64 + b * 32 + r) * ROWB + h * 8;
#pragma unroll
    for (int g = 0; g < 4; ++g) {
      const u32x2 w = {pk_bf16(o[b][4 * g] * rl, o[b][4 * g + 1] * rl), pk_bf16(o[b][4 * g + 2] * rl, o[b][4 * g + 3] * rl)};
      if (g < 3 || h == 0) *(u32x2*)(op + g * 16) = w;
    }
  }
}

__global__ __launch_bounds__(512, 2) void qkn_attn(const char* q_, const char* k_, const char* v_, const unsigned* t_, char* out_) {
  extern __shared__ __attribute__((aligned(16))) char smem[];
  typedef __attribute__((address_space(4))) const unsigned long long* KA;
  KA ka = (KA)__builtin_amdgcn_kernarg_segment_ptr();
  const int bid = blockIdx.x;
  asm volatile(
      "s_load_dwordx8 s[84:91], %0, 0x0\n\t"
      "s_load_dwordx2 s[92:93], %0, 0x20\n\t"
      "s_getpc_b64 s[40:41]\n\t"
      ".Lqk_PC:\n\t"
      "v_readfirstlane_b32 s59, %1\n\t"
      "s_cmp_ge_u32 s59, 192\n\t"
      "s_cbranch_scc1 .Lqk_NCP\n\t"
      "v_lshlrev_b32 v238, 7, %1\n\t"
      "v_min_u32 v238, .Lqk_CODE1-.Lqk_PC-4, v238\n\t"
      "global_load_dword v236, v238, s[40:41]\n\t"
      ".Lqk_NCP:\n\t"
      "s_lshl_b32 s58, %2, 3\n\t"
      "s_and_b32 s58, s58, 56\n\t"
      "s_lshr_b32 s43, %2, 5\n\t"
      "s_add_u32 s58, s58, s43\n\t"
      "v_and_b32 v224, 31, %1\n\t"
      "v_bfe_u32 v238, %1, 5, 1\n\t"
      "s_mul_i32 s54, s58, 0x1c000\n\t"
      "s_bfe_u32 s53, s59, 0x20006\n\t"
      "s_bfe_u32 s55, %2, 0x20003\n\t"
      "v_lshl_or_b32 v227, v224, 1, v238\n\t"
      "s_lshl_b32 s55, s55, 2\n\t"
      "s_add_u32 s55, s55, s53\n\t"
      "s_lshl_b32 s55, s55, 2\n\t"
      "v_mul_u32_u24 v227, 28, v227\n\t"
      "s_lshl_b32 s58, s58, 1\n\t"
      "s_and_b32 s58, s58, 28\n\t"
      "s_lshl_b32 s42, s43, 3\n\t"
      "s_add_u32 s42, s42, s53\n\t"
      "s_cmp_lt_u32 s59, 256\n\t"
      "s_cselect_b32 s55, s55, s42\n\t"
      "s_mul_i32 s55, s55, 1792\n\t"
      "v_bfe_u32 v229, %1, 2, 4\n\t"
      "v_and_b32 v228, 3, %1\n\t"
      "v_mul_u32_u24 v229, 56, v229\n\t"
      "v_lshlrev_b32 v241, 4, v228\n\t"
      "v_min_u32 v241, 40, v241\n\t"
      "v_add_u32 v229, v229, v241\n\t"
      "s_lshl_b32 s52, s53, 12\n\t"
      "s_add_u32 s55, s55, s54\n\t"
      "s_add_u32 m0, s52, 0x800\n\t"
      "s_cmp_lt_u32 s59, 256\n\t"
      "s_waitcnt lgkmcnt(0)\n\t"
      "s_cselect_b32 s40, s84, s86\n\t"
      "s_cselect_b32 s41, s85, s87\n\t"
      "s_cselect_b64 vcc, -1, 0\n\t"
      "s_add_u32 s40, s40, s55\n\t"
      "s_addc_u32 s41, s41, 0\n\t"
      "global_load_dwordx4 v[64:67], v227, s[40:41] offset:0\n\t"
      "global_load_dwordx3 v[68:70], v227, s[40:41] offset:16\n\t"
      "s_load_dword s47, s[90:91], s58\n\t"
      "s_cbranch_vccnz .Lqk_CENTRY\n\t"
      "s_setprio 3\n\t"
      "s_add_u32 s42, s43, 1\n\t"
      "s_add_u32 s50, s88, s55\n\t"
      "s_addc_u32 s51, s89, 0\n\t"
      "global_load_lds_dwordx4 v229, s[50:51]\n\t"
      "s_add_u32 m0, m0, 128\n\t"
      "s_add_u32 s40, s40, 7168\n\t"
      "global_load_lds_dwordx4 v229, s[50:51] offset:896\n\t"
      "s_addc_u32 s41, s41, 0\n\t"
      "global_load_dwordx4 v[72:75], v227, s[40:41] offset:0\n\t"
      "global_load_dwordx3 v[76:78], v227, s[40:41] offset:16\n\t"
      "s_add_u32 s50, s50, 7168\n\t"
      "s_addc_u32 s51, s51, 0\n\t"
      "s_add_u32 m0, m0, 16256\n\t"
      "s_and_b32 s43, s43, 1\n\t"
      "global_load_lds_dwordx4 v229, s[50:51]\n\t"
      "s_add_u32 m0, m0, 128\n\t"
      "s_lshl_b32 s43, s43, 4\n\t"
      "global_load_lds_dwordx4 v229, s[50:51] offset:896\n\t"
      "v_and_b32 v237, 63, %1\n\t"
      "v_lshlrev_b32 v240, 4, v224\n\t"
      "v_lshl_or_b32 v240, v238, 9, v240\n\t"
      "v_add_u32 v240, s52, v240\n\t"
      "v_lshlrev_b32 v241, 6, v224\n\t"
      "v_add_u32 v241, s52, v241\n\t"
      "s_lshl_b32 s52, s52, 1\n\t"
      "v_mul_u32_u24 v226, 56, v237\n\t"
      "v_lshlrev_b32 v238, 12, v238\n\t"
      "v_add_u32 v238, s52, v238\n\t"
      "v_lshl_add_u32 v227, v224, 4, v238\n\t"
      "v_lshl_add_u32 v228, v224, 6, v238\n\t"
      "v_add_u32 v228, 2096, v228\n\t"
      "s_add_u32 s52, s52, 0x800\n\t"
      "s_mul_i32 s55, s53, 3584\n\t"
      "s_add_u32 s55, s55, s54\n\t"
      "s_add_u32 s56, s86, s55\n\t"
      "s_addc_u32 s57, s87, 0\n\t"
      "s_add_u32 s76, s88, s55\n\t"
      "s_addc_u32 s77, s89, 0\n\t"
      "s_and_b32 s42, s42, 7\n\t"
      "s_mul_i32 s42, s42, 14336\n\t"
      "s_mov_b32 s44, 0x8000\n\t"
      "s_mov_b32 s45, 4\n\t"
      "v_mov_b32 v232, 0x3f80\n\t"
      "v_mov_b32 v233, 0\n\t"
      "v_mov_b32 v230, 1.0\n\t"
      "v_mov_b32 v231, 0x358637bd\n\t"
      "v_mov_b32 v238, 0x3f38aa3b\n\t"
      "s_waitcnt lgkmcnt(0)\n\t"
      "s_lshr_b32 s47, s47, s43\n\t"
      "s_and_b32 s58, s47, 0x7fff\n\t"
      "s_sub_u32 s58, s58, 0x3580\n\t"
      "s_cmp_gt_u32 s58, 3072\n\t"
      "s_cbranch_scc1 .Lqk_SLOW\n\t"
      "s_lshl_b32 s47, s47, 16\n\t"
      "v_mul_f32 v238, s47, v238\n\t"
      "v_or_b32 v238, 0x80000000, v238\n\t"
      "v_cvt_pk_bf16_f32 v71, v238, 0\n\t"
      "v_cvt_pk_bf16_f32 v79, v238, 0\n\t"
      "v_cvt_pk_bf16_f32 v87, v238, 0\n\t"
      "v_cvt_pk_bf16_f32 v95, v238, 0\n\t"
      "s_waitcnt vmcnt(6)\n\t"
      "s_call_b64 s[48:49], .Lqk_HN\n\t"
      "ds_write_b128 v240, v[64:67]\n\t"
      "ds_write_b128 v240, v[68:71] offset:1024\n\t"
      "s_waitcnt vmcnt(4)\n\t"
      "ds_write_b64 v241, v[232:233] offset:2096\n\t"
      "s_waitcnt lgkmcnt(0)\n\t"
      "s_barrier\n\t"
      "s_waitcnt vmcnt(2)\n\t"
      "v_dot2_f32_bf16 v242, v72, v72, 0\n\t"
      "v_dot2_f32_bf16 v242, v73, v73, v242\n\t"
      "v_dot2_f32_bf16 v242, v74, v74, v242\n\t"
      "v_dot2_f32_bf16 v242, v75, v75, v242\n\t"
      "v_dot2_f32_bf16 v242, v76, v76, v242\n\t"
      "v_dot2_f32_bf16 v242, v77, v77, v242\n\t"
      "v_dot2_f32_bf16 v242, v78, v78, v242\n\t"
      "s_nop 2\n\t"
      "v_mov_b32 v243, v242\n\t"
      "s_nop 1\n\t"
      "v_permlane32_swap_b32 v243, v242\n\t"
      "v_add_f32 v242, v242, v243\n\t"
      "v_sqrt_f32 v242, v242\n\t"
      "s_nop 0\n\t"
      "v_fma_f32 v242, v242, v230, v231\n\t"
      "v_rcp_f32 v242, v242\n\t"
      "s_nop 0\n\t"
      "v_cvt_pk_bf16_f32 v243, v242, 0\n\t"
      "v_lshlrev_b32 v244, 16, v243\n\t"
      "v_dot2_f32_bf16 v245, v72, v243, 0\n\t"
      "v_dot2_f32_bf16 v246, v72, v244, 0\n\t"
      "v_dot2_f32_bf16 v247, v73, v243, 0\n\t"
      "v_dot2_f32_bf16 v248, v73, v244, 0\n\t"
      "v_dot2_f32_bf16 v249, v74, v243, 0\n\t"
      "v_dot2_f32_bf16 v250, v74, v244, 0\n\t"
      "v_cvt_pk_bf16_f32 v72, v245, v246\n\t"
      "v_dot2_f32_bf16 v245, v75, v243, 0\n\t"
      "v_dot2_f32_bf16 v246, v75, v244, 0\n\t"
      "v_cvt_pk_bf16_f32 v73, v247, v248\n\t"
      "v_dot2_f32_bf16 v247, v76, v243, 0\n\t"
      "v_dot2_f32_bf16 v248, v76, v244, 0\n\t"
      "v_cvt_pk_bf16_f32 v74, v249, v250\n\t"
      "v_dot2_f32_bf16 v249, v77, v243, 0\n\t"
      "v_dot2_f32_bf16 v250, v77, v244, 0\n\t"
      "v_cvt_pk_bf16_f32 v75, v245, v246\n\t"
      "v_dot2_f32_bf16 v245, v78, v243, 0\n\t"
      "v_dot2_f32_bf16 v246, v78, v244, 0\n\t"
      "v_cvt_pk_bf16_f32 v76, v247, v248\n\t"
      "v_cvt_pk_bf16_f32 v77, v249, v250\n\t"
      "s_nop 1\n\t"
      "v_cvt_pk_bf16_f32 v78, v245, v246\n\t"
      "ds_write_b128 v240, v[72:75] offset:16384\n\t"
      "ds_write_b128 v240, v[76:79] offset:17408\n\t"
      "s_waitcnt vmcnt(0)\n\t"
      "ds_write_b64 v241, v[232:233] offset:18480\n\t"
      "s_add_u32 s40, s56, s42\n\t"
      "s_addc_u32 s41, s57, 0\n\t"
      "global_load_dwordx4 v[80:83], v226, s[40:41]\n\t"
      "global_load_dwordx3 v[84:86], v226, s[40:41] offset:16\n\t"
      "global_load_dwordx4 v[88:91], v226, s[40:41] offset:28\n\t"
      "global_load_dwordx3 v[92:94], v226, s[40:41] offset:44\n\t"
      "s_add_u32 s50, s76, s42\n\t"
      "s_addc_u32 s51, s77, 0\n\t"
      "s_add_u32 m0, s52, s44\n\t"
      "s_add_u32 s42, s42, 14336\n\t"
      "global_load_lds_dwordx4 v229, s[50:51]\n\t"
      "s_add_u32 m0, m0, 128\n\t"
      "s_cmp_eq_u32 s42, 114688\n\t"
      "global_load_lds_dwordx4 v229, s[50:51] offset:896\n\t"
      "s_cselect_b32 s42, 0, s42\n\t"
      "s_add_u32 m0, m0, 2176\n\t"
      "s_add_u32 s44, s44, 0x8000\n\t"
      "global_load_lds_dwordx4 v229, s[50:51] offset:1792\n\t"
      "s_add_u32 m0, m0, 128\n\t"
      "s_and_b32 s44, s44, 0x1ffff\n\t"
      "global_load_lds_dwordx4 v229, s[50:51] offset:2688\n\t"
      "s_waitcnt lgkmcnt(0)\n\t"
      "s_barrier\n\t"
      ".Lqk_PL:\n\t"
      "s_add_u32 s40, s56, s42\n\t"
      "s_addc_u32 s41, s57, 0\n\t"
      "global_load_dwordx4 v[64:67], v226, s[40:41]\n\t"
      "global_load_dwordx3 v[68:70], v226, s[40:41] offset:16\n\t"
      "global_load_dwordx4 v[72:75], v226, s[40:41] offset:28\n\t"
      "global_load_dwordx3 v[76:78], v226, s[40:41] offset:44\n\t"
      "s_add_u32 s50, s76, s42\n\t"
      "s_addc_u32 s51, s77, 0\n\t"
      "s_add_u32 m0, s52, s44\n\t"
      "s_add_u32 s42, s42, 14336\n\t"
      "global_load_lds_dwordx4 v229, s[50:51]\n\t"
      "s_add_u32 m0, m0, 128\n\t"
      "s_cmp_eq_u32 s42, 114688\n\t"
      "global_load_lds_dwordx4 v229, s[50:51] offset:896\n\t"
      "s_cselect_b32 s42, 0, s42\n\t"
      "s_add_u32 m0, m0, 2176\n\t"
      "s_add_u32 s44, s44, 0x8000\n\t"
      "global_load_lds_dwordx4 v229, s[50:51] offset:1792\n\t"
      "s_add_u32 m0, m0, 128\n\t"
      "s_and_b32 s44, s44, 0x1ffff\n\t"
      "global_load_lds_dwordx4 v229, s[50:51] offset:2688\n\t"
      "s_waitcnt vmcnt(8)\n\t"
      "ds_write_b64 v228, v[232:233] offset:32768\n\t"
      "v_xor_b32 v228, 0x10000, v228\n\t"
      "s_sub_u32 s45, s45, 1\n\t"
      "s_cmp_eq_u32 s45, 0\n\t"
      "v_dot2_f32_bf16 v242, v80, v80, 0\n\t"
      "v_dot2_f32_bf16 v242, v81, v81, v242\n\t"
      "v_dot2_f32_bf16 v242, v82, v82, v242\n\t"
      "v_dot2_f32_bf16 v242, v83, v83, v242\n\t"
      "v_dot2_f32_bf16 v242, v84, v84, v242\n\t"
      "v_dot2_f32_bf16 v242, v85, v85, v242\n\t"
      "v_dot2_f32_bf16 v242, v86, v86, v242\n\t"
      "v_dot2_f32_bf16 v242, v88, v88, v242\n\t"
      "v_dot2_f32_bf16 v242, v89, v89, v242\n\t"
      "v_dot2_f32_bf16 v242, v90, v90, v242\n\t"
      "v_dot2_f32_bf16 v242, v91, v91, v242\n\t"
      "v_dot2_f32_bf16 v242, v92, v92, v242\n\t"
      "v_dot2_f32_bf16 v242, v93, v93, v242\n\t"
      "v_dot2_f32_bf16 v242, v94, v94, v242\n\t"
      "s_nop 2\n\t"
      "v_sqrt_f32 v242, v242\n\t"
      "s_nop 0\n\t"
      "v_add_f32 v242, v242, v231\n\t"
      "v_rcp_f32 v242, v242\n\t"
      "s_nop 0\n\t"
      "v_cvt_pk_bf16_f32 v243, v242, 0\n\t"
      "v_lshlrev_b32 v244, 16, v243\n\t"
      "v_dot2_f32_bf16 v245, v80, v243, 0\n\t"
      "v_dot2_f32_bf16 v246, v80, v244, 0\n\t"
      "v_dot2_f32_bf16 v247, v81, v243, 0\n\t"
      "v_dot2_f32_bf16 v248, v81, v244, 0\n\t"
      "v_dot2_f32_bf16 v249, v82, v243, 0\n\t"
      "v_dot2_f32_bf16 v250, v82, v244, 0\n\t"
      "v_cvt_pk_bf16_f32 v80, v245, v246\n\t"
      "v_dot2_f32_bf16 v245, v83, v243, 0\n\t"
      "v_dot2_f32_bf16 v246, v83, v244, 0\n\t"
      "v_cvt_pk_bf16_f32 v81, v247, v248\n\t"
      "v_dot2_f32_bf16 v247, v84, v243, 0\n\t"
      "v_dot2_f32_bf16 v248, v84, v244, 0\n\t"
      "v_cvt_pk_bf16_f32 v82, v249, v250\n\t"
      "v_dot2_f32_bf16 v249, v85, v243, 0\n\t"
      "v_dot2_f32_bf16 v250, v85, v244, 0\n\t"
      "v_cvt_pk_bf16_f32 v83, v245, v246\n\t"
      "ds_write_b128 v227, v[80:83] offset:32768\n\t"
      "v_dot2_f32_bf16 v245, v86, v243, 0\n\t"
      "v_dot2_f32_bf16 v246, v86, v244, 0\n\t"
      "v_cvt_pk_bf16_f32 v84, v247, v248\n\t"
      "v_dot2_f32_bf16 v247, v88, v243, 0\n\t"
      "v_dot2_f32_bf16 v248, v88, v244, 0\n\t"
      "v_cvt_pk_bf16_f32 v85, v249, v250\n\t"
      "v_dot2_f32_bf16 v249, v89, v243, 0\n\t"
      "v_dot2_f32_bf16 v250, v89, v244, 0\n\t"
      "v_cvt_pk_bf16_f32 v86, v245, v246\n\t"
      "ds_write_b128 v227, v[84:87] offset:33792\n\t"
      "v_dot2_f32_bf16 v245, v90, v243, 0\n\t"
      "v_dot2_f32_bf16 v246, v90, v244, 0\n\t"
      "v_cvt_pk_bf16_f32 v88, v247, v248\n\t"
      "v_dot2_f32_bf16 v247, v91, v243, 0\n\t"
      "v_dot2_f32_bf16 v248, v91, v244, 0\n\t"
      "v_cvt_pk_bf16_f32 v89, v249, v250\n\t"
      "v_dot2_f32_bf16 v249, v92, v243, 0\n\t"
      "v_dot2_f32_bf16 v250, v92, v244, 0\n\t"
      "v_cvt_pk_bf16_f32 v90, v245, v246\n\t"
      "v_dot2_f32_bf16 v245, v93, v243, 0\n\t"
      "v_dot2_f32_bf16 v246, v93, v244, 0\n\t"
      "v_cvt_pk_bf16_f32 v91, v247, v248\n\t"
      "ds_write_b128 v227, v[88:91] offset:33280\n\t"
      "v_dot2_f32_bf16 v247, v94, v243, 0\n\t"
      "v_dot2_f32_bf16 v248, v94, v244, 0\n\t"
      "v_cvt_pk_bf16_f32 v92, v249, v250\n\t"
      "v_cvt_pk_bf16_f32 v93, v245, v246\n\t"
      "s_nop 1\n\t"
      "v_cvt_pk_bf16_f32 v94, v247, v248\n\t"
      "ds_write_b128 v227, v[92:95] offset:34304\n\t"
      "s_waitcnt lgkmcnt(0)\n\t"
      "s_setprio 0\n\t"
      "s_barrier\n\t"
      "s_cbranch_scc1 .Lqk_PEND\n\t"
      "s_add_u32 s40, s56, s42\n\t"
      "s_addc_u32 s41, s57, 0\n\t"
      "global_load_dwordx4 v[80:83], v226, s[40:41]\n\t"
      "global_load_dwordx3 v[84:86], v226, s[40:41] offset:16\n\t"
      "global_load_dwordx4 v[88:91], v226, s[40:41] offset:28\n\t"
      "global_load_dwordx3 v[92:94], v226, s[40:41] offset:44\n\t"
      "s_add_u32 s50, s76, s42\n\t"
      "s_addc_u32 s51, s77, 0\n\t"
      "s_add_u32 m0, s52, s44\n\t"
      "s_add_u32 s42, s42, 14336\n\t"
      "global_load_lds_dwordx4 v229, s[50:51]\n\t"
      "s_add_u32 m0, m0, 128\n\t"
      "s_cmp_eq_u32 s42, 114688\n\t"
      "global_load_lds_dwordx4 v229, s[50:51] offset:896\n\t"
      "s_cselect_b32 s42, 0, s42\n\t"
      "s_add_u32 m0, m0, 2176\n\t"
      "s_add_u32 s44, s44, 0x8000\n\t"
      "global_load_lds_dwordx4 v229, s[50:51] offset:1792\n\t"
      "s_add_u32 m0, m0, 128\n\t"
      "s_and_b32 s44, s44, 0x1ffff\n\t"
      "global_load_lds_dwordx4 v229, s[50:51] offset:2688\n\t"
      "s_waitcnt vmcnt(8)\n\t"
      "ds_write_b64 v228, v[232:233] offset:0\n\t"
      "v_xor_b32 v227, 0x10000, v227\n\t"
      "v_dot2_f32_bf16 v242, v64, v64, 0\n\t"
      "v_dot2_f32_bf16 v242, v65, v65, v242\n\t"
      "v_dot2_f32_bf16 v242, v66, v66, v242\n\t"
      "v_dot2_f32_bf16 v242, v67, v67, v242\n\t"
      "v_dot2_f32_bf16 v242, v68, v68, v242\n\t"
      "v_dot2_f32_bf16 v242, v69, v69, v242\n\t"
      "v_dot2_f32_bf16 v242, v70, v70, v242\n\t"
      "v_dot2_f32_bf16 v242, v72, v72, v242\n\t"
      "v_dot2_f32_bf16 v242, v73, v73, v242\n\t"
      "v_dot2_f32_bf16 v242, v74, v74, v242\n\t"
      "v_dot2_f32_bf16 v242, v75, v75, v242\n\t"
      "v_dot2_f32_bf16 v242, v76, v76, v242\n\t"
      "v_dot2_f32_bf16 v242, v77, v77, v242\n\t"
      "v_dot2_f32_bf16 v242, v78, v78, v242\n\t"
      "s_nop 2\n\t"
      "v_sqrt_f32 v242, v242\n\t"
      "s_nop 0\n\t"
      "v_add_f32 v242, v242, v231\n\t"
      "v_rcp_f32 v242, v242\n\t"
      "s_nop 0\n\t"
      "v_cvt_pk_bf16_f32 v243, v242, 0\n\t"
      "v_lshlrev_b32 v244, 16, v243\n\t"
      "v_dot2_f32_bf16 v245, v64, v243, 0\n\t"
      "v_dot2_f32_bf16 v246, v64, v244, 0\n\t"
      "v_dot2_f32_bf16 v247, v65, v243, 0\n\t"
      "v_dot2_f32_bf16 v248, v65, v244, 0\n\t"
      "v_dot2_f32_bf16 v249, v66, v243, 0\n\t"
      "v_dot2_f32_bf16 v250, v66, v244, 0\n\t"
      "v_cvt_pk_bf16_f32 v64, v245, v246\n\t"
      "v_dot2_f32_bf16 v245, v67, v243, 0\n\t"
      "v_dot2_f32_bf16 v246, v67, v244, 0\n\t"
      "v_cvt_pk_bf16_f32 v65, v247, v248\n\t"
      "v_dot2_f32_bf16 v247, v68, v243, 0\n\t"
      "v_dot2_f32_bf16 v248, v68, v244, 0\n\t"
      "v_cvt_pk_bf16_f32 v66, v249, v250\n\t"
      "v_dot2_f32_bf16 v249, v69, v243, 0\n\t"
      "v_dot2_f32_bf16 v250, v69, v244, 0\n\t"
      "v_cvt_pk_bf16_f32 v67, v245, v246\n\t"
      "ds_write_b128 v227, v[64:67] offset:0\n\t"
      "v_dot2_f32_bf16 v245, v70, v243, 0\n\t"
      "v_dot2_f32_bf16 v246, v70, v244, 0\n\t"
      "v_cvt_pk_bf16_f32 v68, v247, v248\n\t"
      "v_dot2_f32_bf16 v247, v72, v243, 0\n\t"
      "v_dot2_f32_bf16 v248, v72, v244, 0\n\t"
      "v_cvt_pk_bf16_f32 v69, v249, v250\n\t"
      "v_dot2_f32_bf16 v249, v73, v243, 0\n\t"
      "v_dot2_f32_bf16 v250, v73, v244, 0\n\t"
      "v_cvt_pk_bf16_f32 v70, v245, v246\n\t"
      "ds_write_b128 v227, v[68:71] offset:1024\n\t"
      "v_dot2_f32_bf16 v245, v74, v243, 0\n\t"
      "v_dot2_f32_bf16 v246, v74, v244, 0\n\t"
      "v_cvt_pk_bf16_f32 v72, v247, v248\n\t"
      "v_dot2_f32_bf16 v247, v75, v243, 0\n\t"
      "v_dot2_f32_bf16 v248, v75, v244, 0\n\t"
      "v_cvt_pk_bf16_f32 v73, v249, v250\n\t"
      "v_dot2_f32_bf16 v249, v76, v243, 0\n\t"
      "v_dot2_f32_bf16 v250, v76, v244, 0\n\t"
      "v_cvt_pk_bf16_f32 v74, v245, v246\n\t"
      "v_dot2_f32_bf16 v245, v77, v243, 0\n\t"
      "v_dot2_f32_bf16 v246, v77, v244, 0\n\t"
      "v_cvt_pk_bf16_f32 v75, v247, v248\n\t"
      "ds_write_b128 v227, v[72:75] offset:512\n\t"
      "v_dot2_f32_bf16 v247, v78, v243, 0\n\t"
      "v_dot2_f32_bf16 v248, v78, v244, 0\n\t"
      "v_cvt_pk_bf16_f32 v76, v249, v250\n\t"
      "v_cvt_pk_bf16_f32 v77, v245, v246\n\t"
      "s_nop 1\n\t"
      "v_cvt_pk_bf16_f32 v78, v247, v248\n\t"
      "ds_write_b128 v227, v[76:79] offset:1536\n\t"
      "s_waitcnt lgkmcnt(0)\n\t"
      "s_setprio 0\n\t"
      "s_barrier\n\t"
      "s_branch .Lqk_PL\n\t"
      ".Lqk_PEND:\n\t"
      "s_endpgm\n\t"
      ".Lqk_HN:\n\t"
      "v_dot2_f32_bf16 v242, v64, v64, 0\n\t"
      "v_dot2_f32_bf16 v242, v65, v65, v242\n\t"
      "v_dot2_f32_bf16 v242, v66, v66, v242\n\t"
      "v_dot2_f32_bf16 v242, v67, v67, v242\n\t"
      "v_dot2_f32_bf16 v242, v68, v68, v242\n\t"
      "v_dot2_f32_bf16 v242, v69, v69, v242\n\t"
      "v_dot2_f32_bf16 v242, v70, v70, v242\n\t"
      "s_nop 2\n\t"
      "v_mov_b32 v243, v242\n\t"
      "s_nop 1\n\t"
      "v_permlane32_swap_b32 v243, v242\n\t"
      "v_add_f32 v242, v242, v243\n\t"
      "v_sqrt_f32 v242, v242\n\t"
      "s_nop 0\n\t"
      "v_fma_f32 v242, v242, v230, v231\n\t"
      "v_rcp_f32 v242, v242\n\t"
      "s_nop 0\n\t"
      "v_cvt_pk_bf16_f32 v243, v242, 0\n\t"
      "v_lshlrev_b32 v244, 16, v243\n\t"
      "v_dot2_f32_bf16 v245, v64, v243, 0\n\t"
      "v_dot2_f32_bf16 v246, v64, v244, 0\n\t"
      "v_dot2_f32_bf16 v247, v65, v243, 0\n\t"
      "v_dot2_f32_bf16 v248, v65, v244, 0\n\t"
      "v_dot2_f32_bf16 v249, v66, v243, 0\n\t"
      "v_dot2_f32_bf16 v250, v66, v244, 0\n\t"
      "v_cvt_pk_bf16_f32 v64, v245, v246\n\t"
      "v_dot2_f32_bf16 v245, v67, v243, 0\n\t"
      "v_dot2_f32_bf16 v246, v67, v244, 0\n\t"
      "v_cvt_pk_bf16_f32 v65, v247, v248\n\t"
      "v_dot2_f32_bf16 v247, v68, v243, 0\n\t"
      "v_dot2_f32_bf16 v248, v68, v244, 0\n\t"
      "v_cvt_pk_bf16_f32 v66, v249, v250\n\t"
      "v_dot2_f32_bf16 v249, v69, v243, 0\n\t"
      "v_dot2_f32_bf16 v250, v69, v244, 0\n\t"
      "v_cvt_pk_bf16_f32 v67, v245, v246\n\t"
      "v_dot2_f32_bf16 v245, v70, v243, 0\n\t"
      "v_dot2_f32_bf16 v246, v70, v244, 0\n\t"
      "v_cvt_pk_bf16_f32 v68, v247, v248\n\t"
      "v_cvt_pk_bf16_f32 v69, v249, v250\n\t"
      "s_nop 1\n\t"
      "v_cvt_pk_bf16_f32 v70, v245, v246\n\t"
      "s_setpc_b64 s[48:49]\n\t"
      ".Lqk_CENTRY:\n\t"
      "v_and_b32 v237, 63, %1\n\t"
      "s_and_b32 s43, s43, 1\n\t"
      "s_lshl_b32 s43, s43, 4\n\t"
      "v_bfe_u32 v225, %1, 2, 2\n\t"
      "v_lshl_or_b32 v225, v238, 3, v225\n\t"
      "v_lshlrev_b32 v225, 6, v225\n\t"
      "v_bfe_u32 v241, %1, 4, 1\n\t"
      "v_lshl_or_b32 v225, v241, 5, v225\n\t"
      "v_lshl_or_b32 v225, v228, 3, v225\n\t"
      "v_or_b32 v225, 0x800, v225\n\t"
      "s_lshl_b32 s58, s53, 13\n\t"
      "s_add_u32 s59, s58, 0x8000\n\t"
      "v_mul_u32_u24 v228, 56, v224\n\t"
      "v_lshl_add_u32 v228, v238, 3, v228\n\t"
      "v_add_u32 v228, s59, v228\n\t"
      "v_lshlrev_b32 v224, 4, v224\n\t"
      "v_lshl_or_b32 v224, v238, 9, v224\n\t"
      "v_xor_b32 v238, 1, v238\n\t"
      "v_lshl_add_u32 v229, v238, 15, v228\n\t"
      "v_lshlrev_b32 v241, 4, v237\n\t"
      "v_add_u32 v240, s59, v241\n\t"
      "s_add_u32 s55, s55, 3072\n\t"
      "s_add_u32 s50, s92, s55\n\t"
      "s_addc_u32 s51, s93, 0\n\t"
      "v_mov_b32 v160, 0\n\t"
      "v_mov_b32 v161, 0\n\t"
      "v_mov_b32 v162, 0\n\t"
      "v_mov_b32 v163, 0\n\t"
      "v_mov_b32 v71, 0x3f80\n\t"
      "v_mov_b32 v79, 0x3f80\n\t"
      "v_mov_b32 v87, 0x3f80\n\t"
      "v_mov_b32 v95, 0x3f80\n\t"
      "v_mov_b32 v239, 0xdddddddd\n\t"
      "v_mov_b32 v238, 0x3fb8aa3b\n\t"
      "v_mfma_f32_32x32x16_bf16 v[0:15], v[160:163], v[160:163], 0\n\t"
      "v_mfma_f32_32x32x16_bf16 v[16:31], v[160:163], v[160:163], 0\n\t"
      "v_mfma_f32_32x32x16_bf16 v[32:47], v[160:163], v[160:163], 0\n\t"
      "v_mfma_f32_32x32x16_bf16 v[48:63], v[160:163], v[160:163], 0\n\t"
      "s_add_u32 s40, s40, 3584\n\t"
      "s_addc_u32 s41, s41, 0\n\t"
      "global_load_dwordx4 v[72:75], v227, s[40:41] offset:-1792\n\t"
      "global_load_dwordx3 v[76:78], v227, s[40:41] offset:-1776\n\t"
      "s_mov_b32 s46, 0x8000\n\t"
      "s_mov_b32 s45, 7\n\t"
      "s_mov_b64 vcc, 0\n\t"
      "s_waitcnt lgkmcnt(0)\n\t"
      "s_lshr_b32 s47, s47, s43\n\t"
      "s_and_b32 s58, s47, 0x7fff\n\t"
      "s_sub_u32 s58, s58, 0x3580\n\t"
      "s_cmp_gt_u32 s58, 3072\n\t"
      "s_cbranch_scc1 .Lqk_SLOW\n\t"
      "s_lshl_b32 s47, s47, 16\n\t"
      "v_mul_f32 v238, s47, v238\n\t"
      "v_rcp_f32 v230, v238\n\t"
      "s_nop 0\n\t"
      "v_mul_f32 v231, 0x358637bd, v230\n\t"
      "s_waitcnt vmcnt(2)\n\t"
      "global_load_dwordx4 v[80:83], v227, s[40:41] offset:0\n\t"
      "global_load_dwordx3 v[84:86], v227, s[40:41] offset:16\n\t"
      "global_load_dwordx4 v[88:91], v227, s[40:41] offset:1792\n\t"
      "global_load_dwordx3 v[92:94], v227, s[40:41] offset:1808\n\t"
      "s_call_b64 s[48:49], .Lqk_HN\n\t"
      "s_setprio 2\n\t"
      "s_barrier\n\t"
      "ds_read_b128 v[160:163], v224 offset:0\n\t"
      "ds_read_b128 v[164:167], v224 offset:1024\n\t"
      "ds_read_b128 v[192:195], v224 offset:4096\n\t"
      "ds_read_b128 v[196:199], v224 offset:5120\n\t"
      "ds_read_b64_tr_b16 v[176:177], v225 offset:0\n\t"
      "ds_read_b64_tr_b16 v[178:179], v225 offset:256\n\t"
      "ds_read_b64_tr_b16 v[180:181], v225 offset:1024\n\t"
      "ds_read_b64_tr_b16 v[182:183], v225 offset:1280\n\t"
      "s_waitcnt lgkmcnt(6)\n\t"
      "v_mfma_f32_32x32x16_bf16 v[96:111], v[160:163], v[64:67], 0\n\t"
      "v_mfma_f32_32x32x16_bf16 v[96:111], v[164:167], v[68:71], v[96:111]\n\t"
      "ds_read_b128 v[208:211], v224 offset:8192\n\t"
      "ds_read_b128 v[212:215], v224 offset:9216\n\t"
      "ds_read_b64_tr_b16 v[200:201], v225 offset:4096\n\t"
      "ds_read_b64_tr_b16 v[202:203], v225 offset:4352\n\t"
      "ds_read_b64_tr_b16 v[204:205], v225 offset:5120\n\t"
      "ds_read_b64_tr_b16 v[206:207], v225 offset:5376\n\t"
      "s_waitcnt lgkmcnt(10)\n\t"
      "s_nop 5\n\t"
      "v_exp_f32 v96, v96\n\t"
      "v_exp_f32 v97, v97\n\t"
      "v_exp_f32 v98, v98\n\t"
      "v_exp_f32 v99, v99\n\t"
      "v_mfma_f32_32x32x16_bf16 v[112:127], v[192:195], v[64:67], 0\n\t"
      "ds_read_b128 v[168:171], v224 offset:12288\n\t"
      "v_exp_f32 v100, v100\n\t"
      "v_exp_f32 v101, v101\n\t"
      "v_exp_f32 v102, v102\n\t"
      "v_exp_f32 v103, v103\n\t"
      "v_mfma_f32_32x32x16_bf16 v[112:127], v[196:199], v[68:71], v[112:127]\n\t"
      "ds_read_b128 v[172:175], v224 offset:13312\n\t"
      "v_exp_f32 v104, v104\n\t"
      "v_exp_f32 v105, v105\n\t"
      "v_exp_f32 v106, v106\n\t"
      "v_exp_f32 v107, v107\n\t"
      "ds_read_b64_tr_b16 v[216:217], v225 offset:8192\n\t"
      "v_exp_f32 v108, v108\n\t"
      "v_exp_f32 v109, v109\n\t"
      "v_exp_f32 v110, v110\n\t"
      "v_exp_f32 v111, v111\n\t"
      "ds_read_b64_tr_b16 v[218:219], v225 offset:8448\n\t"
      "s_waitcnt lgkmcnt(9)\n\t"
      "v_exp_f32 v112, v112\n\t"
      "v_exp_f32 v113, v113\n\t"
      "v_exp_f32 v114, v114\n\t"
      "v_exp_f32 v115, v115\n\t"
      "v_mfma_f32_32x32x16_bf16 v[128:143], v[208:211], v[64:67], 0\n\t"
      "ds_read_b64_tr_b16 v[220:221], v225 offset:9216\n\t"
      "s_waitcnt lgkmcnt(9)\n\t"
      "v_exp_f32 v116, v116\n\t"
      "v_exp_f32 v117, v117\n\t"
      "v_exp_f32 v118, v118\n\t"
      "v_exp_f32 v119, v119\n\t"
      "v_mfma_f32_32x32x16_bf16 v[128:143], v[212:215], v[68:71], v[128:143]\n\t"
      "ds_read_b64_tr_b16 v[222:223], v225 offset:9472\n\t"
      "v_exp_f32 v120, v120\n\t"
      "v_exp_f32 v121, v121\n\t"
      "v_exp_f32 v122, v122\n\t"
      "v_exp_f32 v123, v123\n\t"
      "v_smfmac_f32_32x32x32_bf16 v[0:15], v[176:179], v[96:103], v239\n\t"
      "ds_read_b64_tr_b16 v[184:185], v225 offset:12288\n\t"
      "v_exp_f32 v124, v124\n\t"
      "v_exp_f32 v125, v125\n\t"
      "v_exp_f32 v126, v126\n\t"
      "v_exp_f32 v127, v127\n\t"
      "v_smfmac_f32_32x32x32_bf16 v[0:15], v[180:183], v[104:111], v239\n\t"
      "ds_read_b64_tr_b16 v[186:187], v225 offset:12544\n\t"
      "s_waitcnt lgkmcnt(7)\n\t"
      "s_waitcnt vmcnt(4)\n\t"
      "v_exp_f32 v128, v128\n\t"
      "v_exp_f32 v129, v129\n\t"
      "v_exp_f32 v130, v130\n\t"
      "v_exp_f32 v131, v131\n\t"
      "v_dot2_f32_bf16 v242, v72, v72, 0\n\t"
      "v_dot2_f32_bf16 v242, v73, v73, v242\n\t"
      "v_dot2_f32_bf16 v242, v74, v74, v242\n\t"
      "v_dot2_f32_bf16 v242, v75, v75, v242\n\t"
      "v_dot2_f32_bf16 v242, v76, v76, v242\n\t"
      "v_dot2_f32_bf16 v242, v77, v77, v242\n\t"
      "v_dot2_f32_bf16 v242, v78, v78, v242\n\t"
      "v_mfma_f32_32x32x16_bf16 v[144:159], v[168:171], v[64:67], 0\n\t"
      "ds_read_b64_tr_b16 v[188:189], v225 offset:13312\n\t"
      "s_waitcnt lgkmcnt(7)\n\t"
      "v_exp_f32 v132, v132\n\t"
      "v_exp_f32 v133, v133\n\t"
      "v_exp_f32 v134, v134\n\t"
      "v_exp_f32 v135, v135\n\t"
      "v_mov_b32 v243, v242\n\t"
      "v_mfma_f32_32x32x16_bf16 v[144:159], v[172:175], v[68:71], v[144:159]\n\t"
      "ds_read_b64_tr_b16 v[190:191], v225 offset:13568\n\t"
      "v_exp_f32 v136, v136\n\t"
      "v_exp_f32 v137, v137\n\t"
      "v_exp_f32 v138, v138\n\t"
      "v_exp_f32 v139, v139\n\t"
      "v_permlane32_swap_b32 v243, v242\n\t"
      "v_add_f32 v242, v242, v243\n\t"
      "v_sqrt_f32 v242, v242\n\t"
      "v_smfmac_f32_32x32x32_bf16 v[0:15], v[200:203], v[112:119], v239\n\t"
      "v_fma_f32 v242, v242, v230, v231\n\t"
      "v_rcp_f32 v242, v242\n\t"
      "v_exp_f32 v140, v140\n\t"
      "v_exp_f32 v141, v141\n\t"
      "v_exp_f32 v142, v142\n\t"
      "v_exp_f32 v143, v143\n\t"
      "v_cvt_pk_bf16_f32 v243, v242, 0\n\t"
      "v_lshlrev_b32 v244, 16, v243\n\t"
      "v_dot2_f32_bf16 v245, v72, v243, 0\n\t"
      "v_dot2_f32_bf16 v246, v72, v244, 0\n\t"
      "v_dot2_f32_bf16 v247, v73, v243, 0\n\t"
      "v_dot2_f32_bf16 v248, v73, v244, 0\n\t"
      "v_dot2_f32_bf16 v249, v74, v243, 0\n\t"
      "v_dot2_f32_bf16 v250, v74, v244, 0\n\t"
      "v_cvt_pk_bf16_f32 v72, v245, v246\n\t"
      "v_dot2_f32_bf16 v245, v75, v243, 0\n\t"
      "v_dot2_f32_bf16 v246, v75, v244, 0\n\t"
      "v_cvt_pk_bf16_f32 v73, v247, v248\n\t"
      "v_dot2_f32_bf16 v247, v76, v243, 0\n\t"
      "v_dot2_f32_bf16 v248, v76, v244, 0\n\t"
      "v_cvt_pk_bf16_f32 v74, v249, v250\n\t"
      "v_dot2_f32_bf16 v249, v77, v243, 0\n\t"
      "v_dot2_f32_bf16 v250, v77, v244, 0\n\t"
      "v_cvt_pk_bf16_f32 v75, v245, v246\n\t"
      "v_dot2_f32_bf16 v245, v78, v243, 0\n\t"
      "v_dot2_f32_bf16 v246, v78, v244, 0\n\t"
      "v_cvt_pk_bf16_f32 v76, v247, v248\n\t"
      "v_cvt_pk_bf16_f32 v77, v249, v250\n\t"
      "s_nop 1\n\t"
      "v_cvt_pk_bf16_f32 v78, v245, v246\n\t"
      "v_smfmac_f32_32x32x32_bf16 v[0:15], v[204:207], v[120:127], v239\n\t"
      "v_exp_f32 v144, v144\n\t"
      "v_exp_f32 v145, v145\n\t"
      "v_exp_f32 v146, v146\n\t"
      "v_exp_f32 v147, v147\n\t"
      "v_mfma_f32_32x32x16_bf16 v[96:111], v[160:163], v[72:75], 0\n\t"
      "v_exp_f32 v148, v148\n\t"
      "v_exp_f32 v149, v149\n\t"
      "v_exp_f32 v150, v150\n\t"
      "v_exp_f32 v151, v151\n\t"
      "v_mfma_f32_32x32x16_bf16 v[96:111], v[164:167], v[76:79], v[96:111]\n\t"
      "s_waitcnt lgkmcnt(6)\n\t"
      "v_exp_f32 v152, v152\n\t"
      "v_exp_f32 v153, v153\n\t"
      "v_exp_f32 v154, v154\n\t"
      "v_exp_f32 v155, v155\n\t"
      "v_smfmac_f32_32x32x32_bf16 v[0:15], v[216:219], v[128:135], v239\n\t"
      "s_barrier\n\t"
      "s_waitcnt lgkmcnt(4)\n\t"
      "v_exp_f32 v156, v156\n\t"
      "v_exp_f32 v157, v157\n\t"
      "v_exp_f32 v158, v158\n\t"
      "v_exp_f32 v159, v159\n\t"
      "v_smfmac_f32_32x32x32_bf16 v[0:15], v[220:223], v[136:143], v239\n\t"
      "v_exp_f32 v96, v96\n\t"
      "v_exp_f32 v97, v97\n\t"
      "v_exp_f32 v98, v98\n\t"
      "v_exp_f32 v99, v99\n\t"
      "v_mfma_f32_32x32x16_bf16 v[112:127], v[192:195], v[72:75], 0\n\t"
      "v_exp_f32 v100, v100\n\t"
      "v_exp_f32 v101, v101\n\t"
      "v_exp_f32 v102, v102\n\t"
      "v_exp_f32 v103, v103\n\t"
      "v_mfma_f32_32x32x16_bf16 v[112:127], v[196:199], v[76:79], v[112:127]\n\t"
      "s_waitcnt lgkmcnt(2)\n\t"
      "v_exp_f32 v104, v104\n\t"
      "v_exp_f32 v105, v105\n\t"
      "v_exp_f32 v106, v106\n\t"
      "v_exp_f32 v107, v107\n\t"
      "v_smfmac_f32_32x32x32_bf16 v[0:15], v[184:187], v[144:151], v239\n\t"
      "s_waitcnt lgkmcnt(0)\n\t"
      "v_exp_f32 v108, v108\n\t"
      "v_exp_f32 v109, v109\n\t"
      "v_exp_f32 v110, v110\n\t"
      "v_exp_f32 v111, v111\n\t"
      "v_smfmac_f32_32x32x32_bf16 v[0:15], v[188:191], v[152:159], v239\n\t"
      "v_exp_f32 v112, v112\n\t"
      "v_exp_f32 v113, v113\n\t"
      "v_exp_f32 v114, v114\n\t"
      "v_exp_f32 v115, v115\n\t"
      "v_mfma_f32_32x32x16_bf16 v[128:143], v[208:211], v[72:75], 0\n\t"
      "v_exp_f32 v116, v116\n\t"
      "v_exp_f32 v117, v117\n\t"
      "v_exp_f32 v118, v118\n\t"
      "v_exp_f32 v119, v119\n\t"
      "v_mfma_f32_32x32x16_bf16 v[128:143], v[212:215], v[76:79], v[128:143]\n\t"
      "v_exp_f32 v120, v120\n\t"
      "v_exp_f32 v121, v121\n\t"
      "v_exp_f32 v122, v122\n\t"
      "v_exp_f32 v123, v123\n\t"
      "v_smfmac_f32_32x32x32_bf16 v[16:31], v[176:179], v[96:103], v239\n\t"
      "v_exp_f32 v124, v124\n\t"
      "v_exp_f32 v125, v125\n\t"
      "v_exp_f32 v126, v126\n\t"
      "v_exp_f32 v127, v127\n\t"
      "v_smfmac_f32_32x32x32_bf16 v[16:31], v[180:183], v[104:111], v239\n\t"
      "s_waitcnt vmcnt(2)\n\t"
      "v_exp_f32 v128, v128\n\t"
      "v_exp_f32 v129, v129\n\t"
      "v_exp_f32 v130, v130\n\t"
      "v_exp_f32 v131, v131\n\t"
      "v_dot2_f32_bf16 v242, v80, v80, 0\n\t"
      "v_dot2_f32_bf16 v242, v81, v81, v242\n\t"
      "v_dot2_f32_bf16 v242, v82, v82, v242\n\t"
      "v_dot2_f32_bf16 v242, v83, v83, v242\n\t"
      "v_dot2_f32_bf16 v242, v84, v84, v242\n\t"
      "v_dot2_f32_bf16 v242, v85, v85, v242\n\t"
      "v_dot2_f32_bf16 v242, v86, v86, v242\n\t"
      "v_mfma_f32_32x32x16_bf16 v[144:159], v[168:171], v[72:75], 0\n\t"
      "v_exp_f32 v132, v132\n\t"
      "v_exp_f32 v133, v133\n\t"
      "v_exp_f32 v134, v134\n\t"
      "v_exp_f32 v135, v135\n\t"
      "v_mov_b32 v243, v242\n\t"
      "v_mfma_f32_32x32x16_bf16 v[144:159], v[172:175], v[76:79], v[144:159]\n\t"
      "v_exp_f32 v136, v136\n\t"
      "v_exp_f32 v137, v137\n\t"
      "v_exp_f32 v138, v138\n\t"
      "v_exp_f32 v139, v139\n\t"
      "v_permlane32_swap_b32 v243, v242\n\t"
      "v_add_f32 v242, v242, v243\n\t"
      "v_sqrt_f32 v242, v242\n\t"
      "v_smfmac_f32_32x32x32_bf16 v[16:31], v[200:203], v[112:119], v239\n\t"
      "v_fma_f32 v242, v242, v230, v231\n\t"
      "v_rcp_f32 v242, v242\n\t"
      "v_exp_f32 v140, v140\n\t"
      "v_exp_f32 v141, v141\n\t"
      "v_exp_f32 v142, v142\n\t"
      "v_exp_f32 v143, v143\n\t"
      "v_cvt_pk_bf16_f32 v243, v242, 0\n\t"
      "v_lshlrev_b32 v244, 16, v243\n\t"
      "v_dot2_f32_bf16 v245, v80, v243, 0\n\t"
      "v_dot2_f32_bf16 v246, v80, v244, 0\n\t"
      "v_dot2_f32_bf16 v247, v81, v243, 0\n\t"
      "v_dot2_f32_bf16 v248, v81, v244, 0\n\t"
      "v_dot2_f32_bf16 v249, v82, v243, 0\n\t"
      "v_dot2_f32_bf16 v250, v82, v244, 0\n\t"
      "v_cvt_pk_bf16_f32 v80, v245, v246\n\t"
      "v_dot2_f32_bf16 v245, v83, v243, 0\n\t"
      "v_dot2_f32_bf16 v246, v83, v244, 0\n\t"
      "v_cvt_pk_bf16_f32 v81, v247, v248\n\t"
      "v_dot2_f32_bf16 v247, v84, v243, 0\n\t"
      "v_dot2_f32_bf16 v248, v84, v244, 0\n\t"
      "v_cvt_pk_bf16_f32 v82, v249, v250\n\t"
      "v_dot2_f32_bf16 v249, v85, v243, 0\n\t"
      "v_dot2_f32_bf16 v250, v85, v244, 0\n\t"
      "v_cvt_pk_bf16_f32 v83, v245, v246\n\t"
      "v_dot2_f32_bf16 v245, v86, v243, 0\n\t"
      "v_dot2_f32_bf16 v246, v86, v244, 0\n\t"
      "v_cvt_pk_bf16_f32 v84, v247, v248\n\t"
      "v_cvt_pk_bf16_f32 v85, v249, v250\n\t"
      "s_nop 1\n\t"
      "v_cvt_pk_bf16_f32 v86, v245, v246\n\t"
      "v_smfmac_f32_32x32x32_bf16 v[16:31], v[204:207], v[120:127], v239\n\t"
      "v_exp_f32 v144, v144\n\t"
      "v_exp_f32 v145, v145\n\t"
      "v_exp_f32 v146, v146\n\t"
      "v_exp_f32 v147, v147\n\t"
      "v_mfma_f32_32x32x16_bf16 v[96:111], v[160:163], v[80:83], 0\n\t"
      "v_exp_f32 v148, v148\n\t"
      "v_exp_f32 v149, v149\n\t"
      "v_exp_f32 v150, v150\n\t"
      "v_exp_f32 v151, v151\n\t"
      "v_mfma_f32_32x32x16_bf16 v[96:111], v[164:167], v[84:87], v[96:111]\n\t"
      "v_exp_f32 v152, v152\n\t"
      "v_exp_f32 v153, v153\n\t"
      "v_exp_f32 v154, v154\n\t"
      "v_exp_f32 v155, v155\n\t"
      "v_smfmac_f32_32x32x32_bf16 v[16:31], v[216:219], v[128:135], v239\n\t"
      "v_exp_f32 v156, v156\n\t"
      "v_exp_f32 v157, v157\n\t"
      "v_exp_f32 v158, v158\n\t"
      "v_exp_f32 v159, v159\n\t"
      "v_smfmac_f32_32x32x32_bf16 v[16:31], v[220:223], v[136:143], v239\n\t"
      "v_exp_f32 v96, v96\n\t"
      "v_exp_f32 v97, v97\n\t"
      "v_exp_f32 v98, v98\n\t"
      "v_exp_f32 v99, v99\n\t"
      "v_mfma_f32_32x32x16_bf16 v[112:127], v[192:195], v[80:83], 0\n\t"
      "v_exp_f32 v100, v100\n\t"
      "v_exp_f32 v101, v101\n\t"
      "v_exp_f32 v102, v102\n\t"
      "v_exp_f32 v103, v103\n\t"
      "v_mfma_f32_32x32x16_bf16 v[112:127], v[196:199], v[84:87], v[112:127]\n\t"
      "v_exp_f32 v104, v104\n\t"
      "v_exp_f32 v105, v105\n\t"
      "v_exp_f32 v106, v106\n\t"
      "v_exp_f32 v107, v107\n\t"
      "v_smfmac_f32_32x32x32_bf16 v[16:31], v[184:187], v[144:151], v239\n\t"
      "v_exp_f32 v108, v108\n\t"
      "v_exp_f32 v109, v109\n\t"
      "v_exp_f32 v110, v110\n\t"
      "v_exp_f32 v111, v111\n\t"
      "v_smfmac_f32_32x32x32_bf16 v[16:31], v[188:191], v[152:159], v239\n\t"
      "v_exp_f32 v112, v112\n\t"
      "v_exp_f32 v113, v113\n\t"
      "v_exp_f32 v114, v114\n\t"
      "v_exp_f32 v115, v115\n\t"
      "v_mfma_f32_32x32x16_bf16 v[128:143], v[208:211], v[80:83], 0\n\t"
      "v_exp_f32 v116, v116\n\t"
      "v_exp_f32 v117, v117\n\t"
      "v_exp_f32 v118, v118\n\t"
      "v_exp_f32 v119, v119\n\t"
      "v_mfma_f32_32x32x16_bf16 v[128:143], v[212:215], v[84:87], v[128:143]\n\t"
      "v_exp_f32 v120, v120\n\t"
      "v_exp_f32 v121, v121\n\t"
      "v_exp_f32 v122, v122\n\t"
      "v_exp_f32 v123, v123\n\t"
      "v_smfmac_f32_32x32x32_bf16 v[32:47], v[176:179], v[96:103], v239\n\t"
      "v_exp_f32 v124, v124\n\t"
      "v_exp_f32 v125, v125\n\t"
      "v_exp_f32 v126, v126\n\t"
      "v_exp_f32 v127, v127\n\t"
      "v_smfmac_f32_32x32x32_bf16 v[32:47], v[180:183], v[104:111], v239\n\t"
      "s_waitcnt vmcnt(0)\n\t"
      "v_exp_f32 v128, v128\n\t"
      "v_exp_f32 v129, v129\n\t"
      "v_exp_f32 v130, v130\n\t"
      "v_exp_f32 v131, v131\n\t"
      "v_dot2_f32_bf16 v242, v88, v88, 0\n\t"
      "v_dot2_f32_bf16 v242, v89, v89, v242\n\t"
      "v_dot2_f32_bf16 v242, v90, v90, v242\n\t"
      "v_dot2_f32_bf16 v242, v91, v91, v242\n\t"
      "v_dot2_f32_bf16 v242, v92, v92, v242\n\t"
      "v_dot2_f32_bf16 v242, v93, v93, v242\n\t"
      "v_dot2_f32_bf16 v242, v94, v94, v242\n\t"
      "v_mfma_f32_32x32x16_bf16 v[144:159], v[168:171], v[80:83], 0\n\t"
      "v_exp_f32 v132, v132\n\t"
      "v_exp_f32 v133, v133\n\t"
      "v_exp_f32 v134, v134\n\t"
      "v_exp_f32 v135, v135\n\t"
      "v_mov_b32 v243, v242\n\t"
      "v_mfma_f32_32x32x16_bf16 v[144:159], v[172:175], v[84:87], v[144:159]\n\t"
      "v_exp_f32 v136, v136\n\t"
      "v_exp_f32 v137, v137\n\t"
      "v_exp_f32 v138, v138\n\t"
      "v_exp_f32 v139, v139\n\t"
      "v_permlane32_swap_b32 v243, v242\n\t"
      "v_add_f32 v242, v242, v243\n\t"
      "v_sqrt_f32 v242, v242\n\t"
      "v_smfmac_f32_32x32x32_bf16 v[32:47], v[200:203], v[112:119], v239\n\t"
      "v_fma_f32 v242, v242, v230, v231\n\t"
      "v_rcp_f32 v242, v242\n\t"
      "v_exp_f32 v140, v140\n\t"
      "v_exp_f32 v141, v141\n\t"
      "v_exp_f32 v142, v142\n\t"
      "v_exp_f32 v143, v143\n\t"
      "v_cvt_pk_bf16_f32 v243, v242, 0\n\t"
      "v_lshlrev_b32 v244, 16, v243\n\t"
      "v_dot2_f32_bf16 v245, v88, v243, 0\n\t"
      "v_dot2_f32_bf16 v246, v88, v244, 0\n\t"
      "v_dot2_f32_bf16 v247, v89, v243, 0\n\t"
      "v_dot2_f32_bf16 v248, v89, v244, 0\n\t"
      "v_dot2_f32_bf16 v249, v90, v243, 0\n\t"
      "v_dot2_f32_bf16 v250, v90, v244, 0\n\t"
      "v_cvt_pk_bf16_f32 v88, v245, v246\n\t"
      "v_dot2_f32_bf16 v245, v91, v243, 0\n\t"
      "v_dot2_f32_bf16 v246, v91, v244, 0\n\t"
      "v_cvt_pk_bf16_f32 v89, v247, v248\n\t"
      "v_dot2_f32_bf16 v247, v92, v243, 0\n\t"
      "v_dot2_f32_bf16 v248, v92, v244, 0\n\t"
      "v_cvt_pk_bf16_f32 v90, v249, v250\n\t"
      "v_dot2_f32_bf16 v249, v93, v243, 0\n\t"
      "v_dot2_f32_bf16 v250, v93, v244, 0\n\t"
      "v_cvt_pk_bf16_f32 v91, v245, v246\n\t"
      "v_dot2_f32_bf16 v245, v94, v243, 0\n\t"
      "v_dot2_f32_bf16 v246, v94, v244, 0\n\t"
      "v_cvt_pk_bf16_f32 v92, v247, v248\n\t"
      "v_cvt_pk_bf16_f32 v93, v249, v250\n\t"
      "s_nop 1\n\t"
      "v_cvt_pk_bf16_f32 v94, v245, v246\n\t"
      "v_smfmac_f32_32x32x32_bf16 v[32:47], v[204:207], v[120:127], v239\n\t"
      "v_exp_f32 v144, v144\n\t"
      "v_exp_f32 v145, v145\n\t"
      "v_exp_f32 v146, v146\n\t"
      "v_exp_f32 v147, v147\n\t"
      "v_mfma_f32_32x32x16_bf16 v[96:111], v[160:163], v[88:91], 0\n\t"
      "v_exp_f32 v148, v148\n\t"
      "v_exp_f32 v149, v149\n\t"
      "v_exp_f32 v150, v150\n\t"
      "v_exp_f32 v151, v151\n\t"
      "v_mfma_f32_32x32x16_bf16 v[96:111], v[164:167], v[92:95], v[96:111]\n\t"
      "v_exp_f32 v152, v152\n\t"
      "v_exp_f32 v153, v153\n\t"
      "v_exp_f32 v154, v154\n\t"
      "v_exp_f32 v155, v155\n\t"
      "v_smfmac_f32_32x32x32_bf16 v[32:47], v[216:219], v[128:135], v239\n\t"
      "v_exp_f32 v156, v156\n\t"
      "v_exp_f32 v157, v157\n\t"
      "v_exp_f32 v158, v158\n\t"
      "v_exp_f32 v159, v159\n\t"
      "v_smfmac_f32_32x32x32_bf16 v[32:47], v[220:223], v[136:143], v239\n\t"
      "v_exp_f32 v96, v96\n\t"
      "v_exp_f32 v97, v97\n\t"
      "v_exp_f32 v98, v98\n\t"
      "v_exp_f32 v99, v99\n\t"
      "v_mfma_f32_32x32x16_bf16 v[112:127], v[192:195], v[88:91], 0\n\t"
      "ds_read_b128 v[160:163], v224 offset:16384\n\t"
      "v_exp_f32 v100, v100\n\t"
      "v_exp_f32 v101, v101\n\t"
      "v_exp_f32 v102, v102\n\t"
      "v_exp_f32 v103, v103\n\t"
      "v_mfma_f32_32x32x16_bf16 v[112:127], v[196:199], v[92:95], v[112:127]\n\t"
      "ds_read_b128 v[164:167], v224 offset:17408\n\t"
      "v_exp_f32 v104, v104\n\t"
      "v_exp_f32 v105, v105\n\t"
      "v_exp_f32 v106, v106\n\t"
      "v_exp_f32 v107, v107\n\t"
      "v_smfmac_f32_32x32x32_bf16 v[32:47], v[184:187], v[144:151], v239\n\t"
      "v_exp_f32 v108, v108\n\t"
      "v_exp_f32 v109, v109\n\t"
      "v_exp_f32 v110, v110\n\t"
      "v_exp_f32 v111, v111\n\t"
      "v_smfmac_f32_32x32x32_bf16 v[32:47], v[188:191], v[152:159], v239\n\t"
      "v_exp_f32 v112, v112\n\t"
      "v_exp_f32 v113, v113\n\t"
      "v_exp_f32 v114, v114\n\t"
      "v_exp_f32 v115, v115\n\t"
      "v_mfma_f32_32x32x16_bf16 v[128:143], v[208:211], v[88:91], 0\n\t"
      "v_exp_f32 v116, v116\n\t"
      "v_exp_f32 v117, v117\n\t"
      "v_exp_f32 v118, v118\n\t"
      "v_exp_f32 v119, v119\n\t"
      "v_mfma_f32_32x32x16_bf16 v[128:143], v[212:215], v[92:95], v[128:143]\n\t"
      "v_exp_f32 v120, v120\n\t"
      "v_exp_f32 v121, v121\n\t"
      "v_exp_f32 v122, v122\n\t"
      "v_exp_f32 v123, v123\n\t"
      "v_smfmac_f32_32x32x32_bf16 v[48:63], v[176:179], v[96:103], v239\n\t"
      "v_exp_f32 v124, v124\n\t"
      "v_exp_f32 v125, v125\n\t"
      "v_exp_f32 v126, v126\n\t"
      "v_exp_f32 v127, v127\n\t"
      "v_smfmac_f32_32x32x32_bf16 v[48:63], v[180:183], v[104:111], v239\n\t"
      "v_exp_f32 v128, v128\n\t"
      "v_exp_f32 v129, v129\n\t"
      "v_exp_f32 v130, v130\n\t"
      "v_exp_f32 v131, v131\n\t"
      "v_mfma_f32_32x32x16_bf16 v[144:159], v[168:171], v[88:91], 0\n\t"
      "ds_read_b64_tr_b16 v[176:177], v225 offset:16384\n\t"
      "v_exp_f32 v132, v132\n\t"
      "v_exp_f32 v133, v133\n\t"
      "v_exp_f32 v134, v134\n\t"
      "v_exp_f32 v135, v135\n\t"
      "v_mfma_f32_32x32x16_bf16 v[144:159], v[172:175], v[92:95], v[144:159]\n\t"
      "ds_read_b64_tr_b16 v[178:179], v225 offset:16640\n\t"
      "v_exp_f32 v136, v136\n\t"
      "v_exp_f32 v137, v137\n\t"
      "v_exp_f32 v138, v138\n\t"
      "v_exp_f32 v139, v139\n\t"
      "v_smfmac_f32_32x32x32_bf16 v[48:63], v[200:203], v[112:119], v239\n\t"
      "ds_read_b64_tr_b16 v[180:181], v225 offset:17408\n\t"
      "v_exp_f32 v140, v140\n\t"
      "v_exp_f32 v141, v141\n\t"
      "v_exp_f32 v142, v142\n\t"
      "v_exp_f32 v143, v143\n\t"
      "v_smfmac_f32_32x32x32_bf16 v[48:63], v[204:207], v[120:127], v239\n\t"
      "ds_read_b64_tr_b16 v[182:183], v225 offset:17664\n\t"
      "s_waitcnt lgkmcnt(5)\n\t"
      "v_exp_f32 v144, v144\n\t"
      "v_exp_f32 v145, v145\n\t"
      "v_exp_f32 v146, v146\n\t"
      "v_exp_f32 v147, v147\n\t"
      "v_mfma_f32_32x32x16_bf16 v[96:111], v[160:163], v[64:67], 0\n\t"
      "s_waitcnt lgkmcnt(4)\n\t"
      "v_exp_f32 v148, v148\n\t"
      "v_exp_f32 v149, v149\n\t"
      "v_exp_f32 v150, v150\n\t"
      "v_exp_f32 v151, v151\n\t"
      "v_mfma_f32_32x32x16_bf16 v[96:111], v[164:167], v[68:71], v[96:111]\n\t"
      "v_exp_f32 v152, v152\n\t"
      "v_exp_f32 v153, v153\n\t"
      "v_exp_f32 v154, v154\n\t"
      "v_exp_f32 v155, v155\n\t"
      "v_smfmac_f32_32x32x32_bf16 v[48:63], v[216:219], v[128:135], v239\n\t"
      "v_exp_f32 v156, v156\n\t"
      "v_exp_f32 v157, v157\n\t"
      "v_exp_f32 v158, v158\n\t"
      "v_exp_f32 v159, v159\n\t"
      "v_smfmac_f32_32x32x32_bf16 v[48:63], v[220:223], v[136:143], v239\n\t"
      "s_branch .Lqk_MID\n\t"
      ".Lqk_OUTER:\n\t"
      "v_exp_f32 v96, v96\n\t"
      "v_exp_f32 v97, v97\n\t"
      "v_exp_f32 v98, v98\n\t"
      "v_exp_f32 v99, v99\n\t"
      "v_mfma_f32_32x32x16_bf16 v[112:127], v[168:171], v[72:75], 0\n\t"
      "v_exp_f32 v100, v100\n\t"
      "v_exp_f32 v101, v101\n\t"
      "v_exp_f32 v102, v102\n\t"
      "v_exp_f32 v103, v103\n\t"
      "v_mfma_f32_32x32x16_bf16 v[112:127], v[172:175], v[76:79], v[112:127]\n\t"
      "v_exp_f32 v104, v104\n\t"
      "v_exp_f32 v105, v105\n\t"
      "v_exp_f32 v106, v106\n\t"
      "v_exp_f32 v107, v107\n\t"
      "v_smfmac_f32_32x32x32_bf16 v[48:63], v[176:179], v[144:151], v239\n\t"
      "v_exp_f32 v108, v108\n\t"
      "v_exp_f32 v109, v109\n\t"
      "v_exp_f32 v110, v110\n\t"
      "v_exp_f32 v111, v111\n\t"
      "v_smfmac_f32_32x32x32_bf16 v[48:63], v[180:183], v[152:159], v239\n\t"
      "v_exp_f32 v112, v112\n\t"
      "v_exp_f32 v113, v113\n\t"
      "v_exp_f32 v114, v114\n\t"
      "v_exp_f32 v115, v115\n\t"
      "v_mfma_f32_32x32x16_bf16 v[128:143], v[168:171], v[80:83], 0\n\t"
      "v_exp_f32 v116, v116\n\t"
      "v_exp_f32 v117, v117\n\t"
      "v_exp_f32 v118, v118\n\t"
      "v_exp_f32 v119, v119\n\t"
      "v_mfma_f32_32x32x16_bf16 v[128:143], v[172:175], v[84:87], v[128:143]\n\t"
      "s_waitcnt lgkmcnt(0)\n\t"
      "v_exp_f32 v120, v120\n\t"
      "v_exp_f32 v121, v121\n\t"
      "v_exp_f32 v122, v122\n\t"
      "v_exp_f32 v123, v123\n\t"
      "v_smfmac_f32_32x32x32_bf16 v[0:15], v[184:187], v[96:103], v239\n\t"
      "v_exp_f32 v124, v124\n\t"
      "v_exp_f32 v125, v125\n\t"
      "v_exp_f32 v126, v126\n\t"
      "v_exp_f32 v127, v127\n\t"
      "v_smfmac_f32_32x32x32_bf16 v[0:15], v[188:191], v[104:111], v239\n\t"
      "s_barrier\n\t"
      "v_exp_f32 v128, v128\n\t"
      "v_exp_f32 v129, v129\n\t"
      "v_exp_f32 v130, v130\n\t"
      "v_exp_f32 v131, v131\n\t"
      "ds_read_b64_tr_b16 v[176:177], v225 offset:0\n\t"
      "ds_read_b64_tr_b16 v[178:179], v225 offset:256\n\t"
      "ds_read_b128 v[160:163], v224 offset:0\n\t"
      "v_mfma_f32_32x32x16_bf16 v[144:159], v[168:171], v[88:91], 0\n\t"
      "v_exp_f32 v132, v132\n\t"
      "v_exp_f32 v133, v133\n\t"
      "v_exp_f32 v134, v134\n\t"
      "v_exp_f32 v135, v135\n\t"
      "ds_read_b64_tr_b16 v[180:181], v225 offset:1024\n\t"
      "ds_read_b128 v[164:167], v224 offset:1024\n\t"
      "ds_read_b64_tr_b16 v[182:183], v225 offset:1280\n\t"
      "v_mfma_f32_32x32x16_bf16 v[144:159], v[172:175], v[92:95], v[144:159]\n\t"
      "v_exp_f32 v136, v136\n\t"
      "v_exp_f32 v137, v137\n\t"
      "v_exp_f32 v138, v138\n\t"
      "v_exp_f32 v139, v139\n\t"
      "v_smfmac_f32_32x32x32_bf16 v[16:31], v[184:187], v[112:119], v239\n\t"
      "v_exp_f32 v140, v140\n\t"
      "v_exp_f32 v141, v141\n\t"
      "v_exp_f32 v142, v142\n\t"
      "v_exp_f32 v143, v143\n\t"
      "v_smfmac_f32_32x32x32_bf16 v[16:31], v[188:191], v[120:127], v239\n\t"
      "s_waitcnt lgkmcnt(1)\n\t"
      "v_exp_f32 v144, v144\n\t"
      "v_exp_f32 v145, v145\n\t"
      "v_exp_f32 v146, v146\n\t"
      "v_exp_f32 v147, v147\n\t"
      "v_mfma_f32_32x32x16_bf16 v[96:111], v[160:163], v[64:67], 0\n\t"
      "v_exp_f32 v148, v148\n\t"
      "v_exp_f32 v149, v149\n\t"
      "v_exp_f32 v150, v150\n\t"
      "v_exp_f32 v151, v151\n\t"
      "v_mfma_f32_32x32x16_bf16 v[96:111], v[164:167], v[68:71], v[96:111]\n\t"
      "v_exp_f32 v152, v152\n\t"
      "v_exp_f32 v153, v153\n\t"
      "v_exp_f32 v154, v154\n\t"
      "v_exp_f32 v155, v155\n\t"
      "v_smfmac_f32_32x32x32_bf16 v[32:47], v[184:187], v[128:135], v239\n\t"
      "v_exp_f32 v156, v156\n\t"
      "v_exp_f32 v157, v157\n\t"
      "v_exp_f32 v158, v158\n\t"
      "v_exp_f32 v159, v159\n\t"
      "v_smfmac_f32_32x32x32_bf16 v[32:47], v[188:191], v[136:143], v239\n\t"
      "v_exp_f32 v96, v96\n\t"
      "v_exp_f32 v97, v97\n\t"
      "v_exp_f32 v98, v98\n\t"
      "v_exp_f32 v99, v99\n\t"
      "v_mfma_f32_32x32x16_bf16 v[112:127], v[160:163], v[72:75], 0\n\t"
      "s_mov_b32 s46, 0x8000\n\t"
      "v_exp_f32 v100, v100\n\t"
      "v_exp_f32 v101, v101\n\t"
      "v_exp_f32 v102, v102\n\t"
      "v_exp_f32 v103, v103\n\t"
      "v_mfma_f32_32x32x16_bf16 v[112:127], v[164:167], v[76:79], v[112:127]\n\t"
      "v_exp_f32 v104, v104\n\t"
      "v_exp_f32 v105, v105\n\t"
      "v_exp_f32 v106, v106\n\t"
      "v_exp_f32 v107, v107\n\t"
      "v_smfmac_f32_32x32x32_bf16 v[48:63], v[184:187], v[144:151], v239\n\t"
      "v_exp_f32 v108, v108\n\t"
      "v_exp_f32 v109, v109\n\t"
      "v_exp_f32 v110, v110\n\t"
      "v_exp_f32 v111, v111\n\t"
      "v_smfmac_f32_32x32x32_bf16 v[48:63], v[188:191], v[152:159], v239\n\t"
      "v_exp_f32 v112, v112\n\t"
      "v_exp_f32 v113, v113\n\t"
      "v_exp_f32 v114, v114\n\t"
      "v_exp_f32 v115, v115\n\t"
      "v_mfma_f32_32x32x16_bf16 v[128:143], v[160:163], v[80:83], 0\n\t"
      "v_exp_f32 v116, v116\n\t"
      "v_exp_f32 v117, v117\n\t"
      "v_exp_f32 v118, v118\n\t"
      "v_exp_f32 v119, v119\n\t"
      "v_mfma_f32_32x32x16_bf16 v[128:143], v[164:167], v[84:87], v[128:143]\n\t"
      "s_waitcnt lgkmcnt(0)\n\t"
      "v_exp_f32 v120, v120\n\t"
      "v_exp_f32 v121, v121\n\t"
      "v_exp_f32 v122, v122\n\t"
      "v_exp_f32 v123, v123\n\t"
      "v_smfmac_f32_32x32x32_bf16 v[0:15], v[176:179], v[96:103], v239\n\t"
      "v_exp_f32 v124, v124\n\t"
      "v_exp_f32 v125, v125\n\t"
      "v_exp_f32 v126, v126\n\t"
      "v_exp_f32 v127, v127\n\t"
      "v_smfmac_f32_32x32x32_bf16 v[0:15], v[180:183], v[104:111], v239\n\t"
      "v_exp_f32 v128, v128\n\t"
      "v_exp_f32 v129, v129\n\t"
      "v_exp_f32 v130, v130\n\t"
      "v_exp_f32 v131, v131\n\t"
      "ds_read_b64_tr_b16 v[184:185], v225 offset:4096\n\t"
      "ds_read_b64_tr_b16 v[186:187], v225 offset:4352\n\t"
      "ds_read_b128 v[168:171], v224 offset:4096\n\t"
      "v_mfma_f32_32x32x16_bf16 v[144:159], v[160:163], v[88:91], 0\n\t"
      "v_exp_f32 v132, v132\n\t"
      "v_exp_f32 v133, v133\n\t"
      "v_exp_f32 v134, v134\n\t"
      "v_exp_f32 v135, v135\n\t"
      "ds_read_b64_tr_b16 v[188:189], v225 offset:5120\n\t"
      "ds_read_b128 v[172:175], v224 offset:5120\n\t"
      "ds_read_b64_tr_b16 v[190:191], v225 offset:5376\n\t"
      "v_mfma_f32_32x32x16_bf16 v[144:159], v[164:167], v[92:95], v[144:159]\n\t"
      "v_exp_f32 v136, v136\n\t"
      "v_exp_f32 v137, v137\n\t"
      "v_exp_f32 v138, v138\n\t"
      "v_exp_f32 v139, v139\n\t"
      "v_smfmac_f32_32x32x32_bf16 v[16:31], v[176:179], v[112:119], v239\n\t"
      "v_exp_f32 v140, v140\n\t"
      "v_exp_f32 v141, v141\n\t"
      "v_exp_f32 v142, v142\n\t"
      "v_exp_f32 v143, v143\n\t"
      "v_smfmac_f32_32x32x32_bf16 v[16:31], v[180:183], v[120:127], v239\n\t"
      "s_waitcnt lgkmcnt(1)\n\t"
      "v_exp_f32 v144, v144\n\t"
      "v_exp_f32 v145, v145\n\t"
      "v_exp_f32 v146, v146\n\t"
      "v_exp_f32 v147, v147\n\t"
      "v_mfma_f32_32x32x16_bf16 v[96:111], v[168:171], v[64:67], 0\n\t"
      "v_exp_f32 v148, v148\n\t"
      "v_exp_f32 v149, v149\n\t"
      "v_exp_f32 v150, v150\n\t"
      "v_exp_f32 v151, v151\n\t"
      "v_mfma_f32_32x32x16_bf16 v[96:111], v[172:175], v[68:71], v[96:111]\n\t"
      "v_exp_f32 v152, v152\n\t"
      "v_exp_f32 v153, v153\n\t"
      "v_exp_f32 v154, v154\n\t"
      "v_exp_f32 v155, v155\n\t"
      "v_smfmac_f32_32x32x32_bf16 v[32:47], v[176:179], v[128:135], v239\n\t"
      "v_exp_f32 v156, v156\n\t"
      "v_exp_f32 v157, v157\n\t"
      "v_exp_f32 v158, v158\n\t"
      "v_exp_f32 v159, v159\n\t"
      "v_smfmac_f32_32x32x32_bf16 v[32:47], v[180:183], v[136:143], v239\n\t"
      "v_exp_f32 v96, v96\n\t"
      "v_exp_f32 v97, v97\n\t"
      "v_exp_f32 v98, v98\n\t"
      "v_exp_f32 v99, v99\n\t"
      "v_mfma_f32_32x32x16_bf16 v[112:127], v[168:171], v[72:75], 0\n\t"
      "v_exp_f32 v100, v100\n\t"
      "v_exp_f32 v101, v101\n\t"
      "v_exp_f32 v102, v102\n\t"
      "v_exp_f32 v103, v103\n\t"
      "v_mfma_f32_32x32x16_bf16 v[112:127], v[172:175], v[76:79], v[112:127]\n\t"
      "v_exp_f32 v104, v104\n\t"
      "v_exp_f32 v105, v105\n\t"
      "v_exp_f32 v106, v106\n\t"
      "v_exp_f32 v107, v107\n\t"
      "v_smfmac_f32_32x32x32_bf16 v[48:63], v[176:179], v[144:151], v239\n\t"
      "v_exp_f32 v108, v108\n\t"
      "v_exp_f32 v109, v109\n\t"
      "v_exp_f32 v110, v110\n\t"
      "v_exp_f32 v111, v111\n\t"
      "v_smfmac_f32_32x32x32_bf16 v[48:63], v[180:183], v[152:159], v239\n\t"
      "v_exp_f32 v112, v112\n\t"
      "v_exp_f32 v113, v113\n\t"
      "v_exp_f32 v114, v114\n\t"
      "v_exp_f32 v115, v115\n\t"
      "v_mfma_f32_32x32x16_bf16 v[128:143], v[168:171], v[80:83], 0\n\t"
      "v_exp_f32 v116, v116\n\t"
      "v_exp_f32 v117, v117\n\t"
      "v_exp_f32 v118, v118\n\t"
      "v_exp_f32 v119, v119\n\t"
      "v_mfma_f32_32x32x16_bf16 v[128:143], v[172:175], v[84:87], v[128:143]\n\t"
      "s_waitcnt lgkmcnt(0)\n\t"
      "v_exp_f32 v120, v120\n\t"
      "v_exp_f32 v121, v121\n\t"
      "v_exp_f32 v122, v122\n\t"
      "v_exp_f32 v123, v123\n\t"
      "v_smfmac_f32_32x32x32_bf16 v[0:15], v[184:187], v[96:103], v239\n\t"
      "v_exp_f32 v124, v124\n\t"
      "v_exp_f32 v125, v125\n\t"
      "v_exp_f32 v126, v126\n\t"
      "v_exp_f32 v127, v127\n\t"
      "v_smfmac_f32_32x32x32_bf16 v[0:15], v[188:191], v[104:111], v239\n\t"
      "v_exp_f32 v128, v128\n\t"
      "v_exp_f32 v129, v129\n\t"
      "v_exp_f32 v130, v130\n\t"
      "v_exp_f32 v131, v131\n\t"
      "ds_read_b64_tr_b16 v[176:177], v225 offset:8192\n\t"
      "ds_read_b64_tr_b16 v[178:179], v225 offset:8448\n\t"
      "ds_read_b128 v[160:163], v224 offset:8192\n\t"
      "v_mfma_f32_32x32x16_bf16 v[144:159], v[168:171], v[88:91], 0\n\t"
      "v_exp_f32 v132, v132\n\t"
      "v_exp_f32 v133, v133\n\t"
      "v_exp_f32 v134, v134\n\t"
      "v_exp_f32 v135, v135\n\t"
      "ds_read_b64_tr_b16 v[180:181], v225 offset:9216\n\t"
      "ds_read_b128 v[164:167], v224 offset:9216\n\t"
      "ds_read_b64_tr_b16 v[182:183], v225 offset:9472\n\t"
      "v_mfma_f32_32x32x16_bf16 v[144:159], v[172:175], v[92:95], v[144:159]\n\t"
      "v_exp_f32 v136, v136\n\t"
      "v_exp_f32 v137, v137\n\t"
      "v_exp_f32 v138, v138\n\t"
      "v_exp_f32 v139, v139\n\t"
      "v_smfmac_f32_32x32x32_bf16 v[16:31], v[184:187], v[112:119], v239\n\t"
      "v_exp_f32 v140, v140\n\t"
      "v_exp_f32 v141, v141\n\t"
      "v_exp_f32 v142, v142\n\t"
      "v_exp_f32 v143, v143\n\t"
      "v_smfmac_f32_32x32x32_bf16 v[16:31], v[188:191], v[120:127], v239\n\t"
      "s_waitcnt lgkmcnt(1)\n\t"
      "v_exp_f32 v144, v144\n\t"
      "v_exp_f32 v145, v145\n\t"
      "v_exp_f32 v146, v146\n\t"
      "v_exp_f32 v147, v147\n\t"
      "v_mfma_f32_32x32x16_bf16 v[96:111], v[160:163], v[64:67], 0\n\t"
      "v_exp_f32 v148, v148\n\t"
      "v_exp_f32 v149, v149\n\t"
      "v_exp_f32 v150, v150\n\t"
      "v_exp_f32 v151, v151\n\t"
      "v_mfma_f32_32x32x16_bf16 v[96:111], v[164:167], v[68:71], v[96:111]\n\t"
      "v_exp_f32 v152, v152\n\t"
      "v_exp_f32 v153, v153\n\t"
      "v_exp_f32 v154, v154\n\t"
      "v_exp_f32 v155, v155\n\t"
      "v_smfmac_f32_32x32x32_bf16 v[32:47], v[184:187], v[128:135], v239\n\t"
      "v_exp_f32 v156, v156\n\t"
      "v_exp_f32 v157, v157\n\t"
      "v_exp_f32 v158, v158\n\t"
      "v_exp_f32 v159, v159\n\t"
      "v_smfmac_f32_32x32x32_bf16 v[32:47], v[188:191], v[136:143], v239\n\t"
      "v_exp_f32 v96, v96\n\t"
      "v_exp_f32 v97, v97\n\t"
      "v_exp_f32 v98, v98\n\t"
      "v_exp_f32 v99, v99\n\t"
      "v_mfma_f32_32x32x16_bf16 v[112:127], v[160:163], v[72:75], 0\n\t"
      "v_exp_f32 v100, v100\n\t"
      "v_exp_f32 v101, v101\n\t"
      "v_exp_f32 v102, v102\n\t"
      "v_exp_f32 v103, v103\n\t"
      "v_mfma_f32_32x32x16_bf16 v[112:127], v[164:167], v[76:79], v[112:127]\n\t"
      "v_exp_f32 v104, v104\n\t"
      "v_exp_f32 v105, v105\n\t"
      "v_exp_f32 v106, v106\n\t"
      "v_exp_f32 v107, v107\n\t"
      "v_smfmac_f32_32x32x32_bf16 v[48:63], v[184:187], v[144:151], v239\n\t"
      "v_exp_f32 v108, v108\n\t"
      "v_exp_f32 v109, v109\n\t"
      "v_exp_f32 v110, v110\n\t"
      "v_exp_f32 v111, v111\n\t"
      "v_smfmac_f32_32x32x32_bf16 v[48:63], v[188:191], v[152:159], v239\n\t"
      "v_exp_f32 v112, v112\n\t"
      "v_exp_f32 v113, v113\n\t"
      "v_exp_f32 v114, v114\n\t"
      "v_exp_f32 v115, v115\n\t"
      "v_mfma_f32_32x32x16_bf16 v[128:143], v[160:163], v[80:83], 0\n\t"
      "v_exp_f32 v116, v116\n\t"
      "v_exp_f32 v117, v117\n\t"
      "v_exp_f32 v118, v118\n\t"
      "v_exp_f32 v119, v119\n\t"
      "v_mfma_f32_32x32x16_bf16 v[128:143], v[164:167], v[84:87], v[128:143]\n\t"
      "s_waitcnt lgkmcnt(0)\n\t"
      "v_exp_f32 v120, v120\n\t"
      "v_exp_f32 v121, v121\n\t"
      "v_exp_f32 v122, v122\n\t"
      "v_exp_f32 v123, v123\n\t"
      "v_smfmac_f32_32x32x32_bf16 v[0:15], v[176:179], v[96:103], v239\n\t"
      "v_exp_f32 v124, v124\n\t"
      "v_exp_f32 v125, v125\n\t"
      "v_exp_f32 v126, v126\n\t"
      "v_exp_f32 v127, v127\n\t"
      "v_smfmac_f32_32x32x32_bf16 v[0:15], v[180:183], v[104:111], v239\n\t"
      "v_exp_f32 v128, v128\n\t"
      "v_exp_f32 v129, v129\n\t"
      "v_exp_f32 v130, v130\n\t"
      "v_exp_f32 v131, v131\n\t"
      "ds_read_b64_tr_b16 v[184:185], v225 offset:12288\n\t"
      "ds_read_b64_tr_b16 v[186:187], v225 offset:12544\n\t"
      "ds_read_b128 v[168:171], v224 offset:12288\n\t"
      "v_mfma_f32_32x32x16_bf16 v[144:159], v[160:163], v[88:91], 0\n\t"
      "v_exp_f32 v132, v132\n\t"
      "v_exp_f32 v133, v133\n\t"
      "v_exp_f32 v134, v134\n\t"
      "v_exp_f32 v135, v135\n\t"
      "ds_read_b64_tr_b16 v[188:189], v225 offset:13312\n\t"
      "ds_read_b128 v[172:175], v224 offset:13312\n\t"
      "ds_read_b64_tr_b16 v[190:191], v225 offset:13568\n\t"
      "v_mfma_f32_32x32x16_bf16 v[144:159], v[164:167], v[92:95], v[144:159]\n\t"
      "v_exp_f32 v136, v136\n\t"
      "v_exp_f32 v137, v137\n\t"
      "v_exp_f32 v138, v138\n\t"
      "v_exp_f32 v139, v139\n\t"
      "v_smfmac_f32_32x32x32_bf16 v[16:31], v[176:179], v[112:119], v239\n\t"
      "v_exp_f32 v140, v140\n\t"
      "v_exp_f32 v141, v141\n\t"
      "v_exp_f32 v142, v142\n\t"
      "v_exp_f32 v143, v143\n\t"
      "v_smfmac_f32_32x32x32_bf16 v[16:31], v[180:183], v[120:127], v239\n\t"
      "s_waitcnt lgkmcnt(1)\n\t"
      "v_exp_f32 v144, v144\n\t"
      "v_exp_f32 v145, v145\n\t"
      "v_exp_f32 v146, v146\n\t"
      "v_exp_f32 v147, v147\n\t"
      "v_mfma_f32_32x32x16_bf16 v[96:111], v[168:171], v[64:67], 0\n\t"
      "v_exp_f32 v148, v148\n\t"
      "v_exp_f32 v149, v149\n\t"
      "v_exp_f32 v150, v150\n\t"
      "v_exp_f32 v151, v151\n\t"
      "v_mfma_f32_32x32x16_bf16 v[96:111], v[172:175], v[68:71], v[96:111]\n\t"
      "v_exp_f32 v152, v152\n\t"
      "v_exp_f32 v153, v153\n\t"
      "v_exp_f32 v154, v154\n\t"
      "v_exp_f32 v155, v155\n\t"
      "v_smfmac_f32_32x32x32_bf16 v[32:47], v[176:179], v[128:135], v239\n\t"
      "v_exp_f32 v156, v156\n\t"
      "v_exp_f32 v157, v157\n\t"
      "v_exp_f32 v158, v158\n\t"
      "v_exp_f32 v159, v159\n\t"
      "v_smfmac_f32_32x32x32_bf16 v[32:47], v[180:183], v[136:143], v239\n\t"
      "v_exp_f32 v96, v96\n\t"
      "v_exp_f32 v97, v97\n\t"
      "v_exp_f32 v98, v98\n\t"
      "v_exp_f32 v99, v99\n\t"
      "v_mfma_f32_32x32x16_bf16 v[112:127], v[168:171], v[72:75], 0\n\t"
      "v_exp_f32 v100, v100\n\t"
      "v_exp_f32 v101, v101\n\t"
      "v_exp_f32 v102, v102\n\t"
      "v_exp_f32 v103, v103\n\t"
      "v_mfma_f32_32x32x16_bf16 v[112:127], v[172:175], v[76:79], v[112:127]\n\t"
      "v_exp_f32 v104, v104\n\t"
      "v_exp_f32 v105, v105\n\t"
      "v_exp_f32 v106, v106\n\t"
      "v_exp_f32 v107, v107\n\t"
      "v_smfmac_f32_32x32x32_bf16 v[48:63], v[176:179], v[144:151], v239\n\t"
      "v_exp_f32 v108, v108\n\t"
      "v_exp_f32 v109, v109\n\t"
      "v_exp_f32 v110, v110\n\t"
      "v_exp_f32 v111, v111\n\t"
      "v_smfmac_f32_32x32x32_bf16 v[48:63], v[180:183], v[152:159], v239\n\t"
      "v_exp_f32 v112, v112\n\t"
      "v_exp_f32 v113, v113\n\t"
      "v_exp_f32 v114, v114\n\t"
      "v_exp_f32 v115, v115\n\t"
      "v_mfma_f32_32x32x16_bf16 v[128:143], v[168:171], v[80:83], 0\n\t"
      "v_exp_f32 v116, v116\n\t"
      "v_exp_f32 v117, v117\n\t"
      "v_exp_f32 v118, v118\n\t"
      "v_exp_f32 v119, v119\n\t"
      "v_mfma_f32_32x32x16_bf16 v[128:143], v[172:175], v[84:87], v[128:143]\n\t"
      "s_waitcnt lgkmcnt(0)\n\t"
      "v_exp_f32 v120, v120\n\t"
      "v_exp_f32 v121, v121\n\t"
      "v_exp_f32 v122, v122\n\t"
      "v_exp_f32 v123, v123\n\t"
      "v_smfmac_f32_32x32x32_bf16 v[0:15], v[184:187], v[96:103], v239\n\t"
      "v_exp_f32 v124, v124\n\t"
      "v_exp_f32 v125, v125\n\t"
      "v_exp_f32 v126, v126\n\t"
      "v_exp_f32 v127, v127\n\t"
      "v_smfmac_f32_32x32x32_bf16 v[0:15], v[188:191], v[104:111], v239\n\t"
      "v_exp_f32 v128, v128\n\t"
      "v_exp_f32 v129, v129\n\t"
      "v_exp_f32 v130, v130\n\t"
      "v_exp_f32 v131, v131\n\t"
      "ds_read_b64_tr_b16 v[176:177], v225 offset:16384\n\t"
      "ds_read_b64_tr_b16 v[178:179], v225 offset:16640\n\t"
      "ds_read_b128 v[160:163], v224 offset:16384\n\t"
      "v_mfma_f32_32x32x16_bf16 v[144:159], v[168:171], v[88:91], 0\n\t"
      "v_exp_f32 v132, v132\n\t"
      "v_exp_f32 v133, v133\n\t"
      "v_exp_f32 v134, v134\n\t"
      "v_exp_f32 v135, v135\n\t"
      "ds_read_b64_tr_b16 v[180:181], v225 offset:17408\n\t"
      "ds_read_b128 v[164:167], v224 offset:17408\n\t"
      "ds_read_b64_tr_b16 v[182:183], v225 offset:17664\n\t"
      "v_mfma_f32_32x32x16_bf16 v[144:159], v[172:175], v[92:95], v[144:159]\n\t"
      "v_exp_f32 v136, v136\n\t"
      "v_exp_f32 v137, v137\n\t"
      "v_exp_f32 v138, v138\n\t"
      "v_exp_f32 v139, v139\n\t"
      "v_smfmac_f32_32x32x32_bf16 v[16:31], v[184:187], v[112:119], v239\n\t"
      "v_exp_f32 v140, v140\n\t"
      "v_exp_f32 v141, v141\n\t"
      "v_exp_f32 v142, v142\n\t"
      "v_exp_f32 v143, v143\n\t"
      "v_smfmac_f32_32x32x32_bf16 v[16:31], v[188:191], v[120:127], v239\n\t"
      "s_waitcnt lgkmcnt(1)\n\t"
      "v_exp_f32 v144, v144\n\t"
      "v_exp_f32 v145, v145\n\t"
      "v_exp_f32 v146, v146\n\t"
      "v_exp_f32 v147, v147\n\t"
      "v_mfma_f32_32x32x16_bf16 v[96:111], v[160:163], v[64:67], 0\n\t"
      "v_exp_f32 v148, v148\n\t"
      "v_exp_f32 v149, v149\n\t"
      "v_exp_f32 v150, v150\n\t"
      "v_exp_f32 v151, v151\n\t"
      "v_mfma_f32_32x32x16_bf16 v[96:111], v[164:167], v[68:71], v[96:111]\n\t"
      "v_exp_f32 v152, v152\n\t"
      "v_exp_f32 v153, v153\n\t"
      "v_exp_f32 v154, v154\n\t"
      "v_exp_f32 v155, v155\n\t"
      "v_smfmac_f32_32x32x32_bf16 v[32:47], v[184:187], v[128:135], v239\n\t"
      "v_exp_f32 v156, v156\n\t"
      "v_exp_f32 v157, v157\n\t"
      "v_exp_f32 v158, v158\n\t"
      "v_exp_f32 v159, v159\n\t"
      "v_smfmac_f32_32x32x32_bf16 v[32:47], v[188:191], v[136:143], v239\n\t"
      "s_cbranch_vccnz .Lqk_TAIL\n\t"
      ".Lqk_MID:\n\t"
      "v_exp_f32 v96, v96\n\t"
      "v_exp_f32 v97, v97\n\t"
      "v_exp_f32 v98, v98\n\t"
      "v_exp_f32 v99, v99\n\t"
      "v_mfma_f32_32x32x16_bf16 v[112:127], v[160:163], v[72:75], 0\n\t"
      "v_exp_f32 v100, v100\n\t"
      "v_exp_f32 v101, v101\n\t"
      "v_exp_f32 v102, v102\n\t"
      "v_exp_f32 v103, v103\n\t"
      "v_mfma_f32_32x32x16_bf16 v[112:127], v[164:167], v[76:79], v[112:127]\n\t"
      "v_exp_f32 v104, v104\n\t"
      "v_exp_f32 v105, v105\n\t"
      "v_exp_f32 v106, v106\n\t"
      "v_exp_f32 v107, v107\n\t"
      "v_smfmac_f32_32x32x32_bf16 v[48:63], v[184:187], v[144:151], v239\n\t"
      "v_exp_f32 v108, v108\n\t"
      "v_exp_f32 v109, v109\n\t"
      "v_exp_f32 v110, v110\n\t"
      "v_exp_f32 v111, v111\n\t"
      "v_smfmac_f32_32x32x32_bf16 v[48:63], v[188:191], v[152:159], v239\n\t"
      "v_exp_f32 v112, v112\n\t"
      "v_exp_f32 v113, v113\n\t"
      "v_exp_f32 v114, v114\n\t"
      "v_exp_f32 v115, v115\n\t"
      "v_mfma_f32_32x32x16_bf16 v[128:143], v[160:163], v[80:83], 0\n\t"
      "v_exp_f32 v116, v116\n\t"
      "v_exp_f32 v117, v117\n\t"
      "v_exp_f32 v118, v118\n\t"
      "v_exp_f32 v119, v119\n\t"
      "v_mfma_f32_32x32x16_bf16 v[128:143], v[164:167], v[84:87], v[128:143]\n\t"
      "s_waitcnt lgkmcnt(0)\n\t"
      "v_exp_f32 v120, v120\n\t"
      "v_exp_f32 v121, v121\n\t"
      "v_exp_f32 v122, v122\n\t"
      "v_exp_f32 v123, v123\n\t"
      "v_smfmac_f32_32x32x32_bf16 v[0:15], v[176:179], v[96:103], v239\n\t"
      "v_exp_f32 v124, v124\n\t"
      "v_exp_f32 v125, v125\n\t"
      "v_exp_f32 v126, v126\n\t"
      "v_exp_f32 v127, v127\n\t"
      "v_smfmac_f32_32x32x32_bf16 v[0:15], v[180:183], v[104:111], v239\n\t"
      "v_exp_f32 v128, v128\n\t"
      "v_exp_f32 v129, v129\n\t"
      "v_exp_f32 v130, v130\n\t"
      "v_exp_f32 v131, v131\n\t"
      "ds_read_b64_tr_b16 v[184:185], v225 offset:20480\n\t"
      "ds_read_b64_tr_b16 v[186:187], v225 offset:20736\n\t"
      "ds_read_b128 v[168:171], v224 offset:20480\n\t"
      "v_mfma_f32_32x32x16_bf16 v[144:159], v[160:163], v[88:91], 0\n\t"
      "v_exp_f32 v132, v132\n\t"
      "v_exp_f32 v133, v133\n\t"
      "v_exp_f32 v134, v134\n\t"
      "v_exp_f32 v135, v135\n\t"
      "ds_read_b64_tr_b16 v[188:189], v225 offset:21504\n\t"
      "ds_read_b128 v[172:175], v224 offset:21504\n\t"
      "ds_read_b64_tr_b16 v[190:191], v225 offset:21760\n\t"
      "v_mfma_f32_32x32x16_bf16 v[144:159], v[164:167], v[92:95], v[144:159]\n\t"
      "v_exp_f32 v136, v136\n\t"
      "v_exp_f32 v137, v137\n\t"
      "v_exp_f32 v138, v138\n\t"
      "v_exp_f32 v139, v139\n\t"
      "v_smfmac_f32_32x32x32_bf16 v[16:31], v[176:179], v[112:119], v239\n\t"
      "v_exp_f32 v140, v140\n\t"
      "v_exp_f32 v141, v141\n\t"
      "v_exp_f32 v142, v142\n\t"
      "v_exp_f32 v143, v143\n\t"
      "v_smfmac_f32_32x32x32_bf16 v[16:31], v[180:183], v[120:127], v239\n\t"
      "s_waitcnt lgkmcnt(1)\n\t"
      "v_exp_f32 v144, v144\n\t"
      "v_exp_f32 v145, v145\n\t"
      "v_exp_f32 v146, v146\n\t"
      "v_exp_f32 v147, v147\n\t"
      "v_mfma_f32_32x32x16_bf16 v[96:111], v[168:171], v[64:67], 0\n\t"
      "v_exp_f32 v148, v148\n\t"
      "v_exp_f32 v149, v149\n\t"
      "v_exp_f32 v150, v150\n\t"
      "v_exp_f32 v151, v151\n\t"
      "v_mfma_f32_32x32x16_bf16 v[96:111], v[172:175], v[68:71], v[96:111]\n\t"
      "v_exp_f32 v152, v152\n\t"
      "v_exp_f32 v153, v153\n\t"
      "v_exp_f32 v154, v154\n\t"
      "v_exp_f32 v155, v155\n\t"
      "v_smfmac_f32_32x32x32_bf16 v[32:47], v[176:179], v[128:135], v239\n\t"
      "v_exp_f32 v156, v156\n\t"
      "v_exp_f32 v157, v157\n\t"
      "v_exp_f32 v158, v158\n\t"
      "v_exp_f32 v159, v159\n\t"
      "v_smfmac_f32_32x32x32_bf16 v[32:47], v[180:183], v[136:143], v239\n\t"
      "v_exp_f32 v96, v96\n\t"
      "v_exp_f32 v97, v97\n\t"
      "v_exp_f32 v98, v98\n\t"
      "v_exp_f32 v99, v99\n\t"
      "v_mfma_f32_32x32x16_bf16 v[112:127], v[168:171], v[72:75], 0\n\t"
      "v_exp_f32 v100, v100\n\t"
      "v_exp_f32 v101, v101\n\t"
      "v_exp_f32 v102, v102\n\t"
      "v_exp_f32 v103, v103\n\t"
      "v_mfma_f32_32x32x16_bf16 v[112:127], v[172:175], v[76:79], v[112:127]\n\t"
      "v_exp_f32 v104, v104\n\t"
      "v_exp_f32 v105, v105\n\t"
      "v_exp_f32 v106, v106\n\t"
      "v_exp_f32 v107, v107\n\t"
      "v_smfmac_f32_32x32x32_bf16 v[48:63], v[176:179], v[144:151], v239\n\t"
      "v_exp_f32 v108, v108\n\t"
      "v_exp_f32 v109, v109\n\t"
      "v_exp_f32 v110, v110\n\t"
      "v_exp_f32 v111, v111\n\t"
      "v_smfmac_f32_32x32x32_bf16 v[48:63], v[180:183], v[152:159], v239\n\t"
      "v_exp_f32 v112, v112\n\t"
      "v_exp_f32 v113, v113\n\t"
      "v_exp_f32 v114, v114\n\t"
      "v_exp_f32 v115, v115\n\t"
      "v_mfma_f32_32x32x16_bf16 v[128:143], v[168:171], v[80:83], 0\n\t"
      "v_exp_f32 v116, v116\n\t"
      "v_exp_f32 v117, v117\n\t"
      "v_exp_f32 v118, v118\n\t"
      "v_exp_f32 v119, v119\n\t"
      "v_mfma_f32_32x32x16_bf16 v[128:143], v[172:175], v[84:87], v[128:143]\n\t"
      "s_waitcnt lgkmcnt(0)\n\t"
      "v_exp_f32 v120, v120\n\t"
      "v_exp_f32 v121, v121\n\t"
      "v_exp_f32 v122, v122\n\t"
      "v_exp_f32 v123, v123\n\t"
      "v_smfmac_f32_32x32x32_bf16 v[0:15], v[184:187], v[96:103], v239\n\t"
      "v_exp_f32 v124, v124\n\t"
      "v_exp_f32 v125, v125\n\t"
      "v_exp_f32 v126, v126\n\t"
      "v_exp_f32 v127, v127\n\t"
      "v_smfmac_f32_32x32x32_bf16 v[0:15], v[188:191], v[104:111], v239\n\t"
      "v_exp_f32 v128, v128\n\t"
      "v_exp_f32 v129, v129\n\t"
      "v_exp_f32 v130, v130\n\t"
      "v_exp_f32 v131, v131\n\t"
      "ds_read_b64_tr_b16 v[176:177], v225 offset:24576\n\t"
      "ds_read_b64_tr_b16 v[178:179], v225 offset:24832\n\t"
      "ds_read_b128 v[160:163], v224 offset:24576\n\t"
      "v_mfma_f32_32x32x16_bf16 v[144:159], v[168:171], v[88:91], 0\n\t"
      "v_exp_f32 v132, v132\n\t"
      "v_exp_f32 v133, v133\n\t"
      "v_exp_f32 v134, v134\n\t"
      "v_exp_f32 v135, v135\n\t"
      "ds_read_b64_tr_b16 v[180:181], v225 offset:25600\n\t"
      "ds_read_b128 v[164:167], v224 offset:25600\n\t"
      "ds_read_b64_tr_b16 v[182:183], v225 offset:25856\n\t"
      "v_mfma_f32_32x32x16_bf16 v[144:159], v[172:175], v[92:95], v[144:159]\n\t"
      "v_exp_f32 v136, v136\n\t"
      "v_exp_f32 v137, v137\n\t"
      "v_exp_f32 v138, v138\n\t"
      "v_exp_f32 v139, v139\n\t"
      "v_smfmac_f32_32x32x32_bf16 v[16:31], v[184:187], v[112:119], v239\n\t"
      "v_exp_f32 v140, v140\n\t"
      "v_exp_f32 v141, v141\n\t"
      "v_exp_f32 v142, v142\n\t"
      "v_exp_f32 v143, v143\n\t"
      "v_smfmac_f32_32x32x32_bf16 v[16:31], v[188:191], v[120:127], v239\n\t"
      "s_waitcnt lgkmcnt(1)\n\t"
      "v_exp_f32 v144, v144\n\t"
      "v_exp_f32 v145, v145\n\t"
      "v_exp_f32 v146, v146\n\t"
      "v_exp_f32 v147, v147\n\t"
      "v_mfma_f32_32x32x16_bf16 v[96:111], v[160:163], v[64:67], 0\n\t"
      "v_exp_f32 v148, v148\n\t"
      "v_exp_f32 v149, v149\n\t"
      "v_exp_f32 v150, v150\n\t"
      "v_exp_f32 v151, v151\n\t"
      "v_mfma_f32_32x32x16_bf16 v[96:111], v[164:167], v[68:71], v[96:111]\n\t"
      "v_exp_f32 v152, v152\n\t"
      "v_exp_f32 v153, v153\n\t"
      "v_exp_f32 v154, v154\n\t"
      "v_exp_f32 v155, v155\n\t"
      "v_smfmac_f32_32x32x32_bf16 v[32:47], v[184:187], v[128:135], v239\n\t"
      "v_exp_f32 v156, v156\n\t"
      "v_exp_f32 v157, v157\n\t"
      "v_exp_f32 v158, v158\n\t"
      "v_exp_f32 v159, v159\n\t"
      "v_smfmac_f32_32x32x32_bf16 v[32:47], v[188:191], v[136:143], v239\n\t"
      "v_exp_f32 v96, v96\n\t"
      "v_exp_f32 v97, v97\n\t"
      "v_exp_f32 v98, v98\n\t"
      "v_exp_f32 v99, v99\n\t"
      "v_mfma_f32_32x32x16_bf16 v[112:127], v[160:163], v[72:75], 0\n\t"
      "s_sub_u32 s45, s45, 1\n\t"
      "v_exp_f32 v100, v100\n\t"
      "v_exp_f32 v101, v101\n\t"
      "v_exp_f32 v102, v102\n\t"
      "v_exp_f32 v103, v103\n\t"
      "v_mfma_f32_32x32x16_bf16 v[112:127], v[164:167], v[76:79], v[112:127]\n\t"
      "s_cmp_eq_u32 s45, 3\n\t"
      "v_exp_f32 v104, v104\n\t"
      "v_exp_f32 v105, v105\n\t"
      "v_exp_f32 v106, v106\n\t"
      "v_exp_f32 v107, v107\n\t"
      "v_smfmac_f32_32x32x32_bf16 v[48:63], v[184:187], v[144:151], v239\n\t"
      "s_cselect_b32 s46, 0xfffe8000, s46\n\t"
      "v_exp_f32 v108, v108\n\t"
      "v_exp_f32 v109, v109\n\t"
      "v_exp_f32 v110, v110\n\t"
      "v_exp_f32 v111, v111\n\t"
      "v_smfmac_f32_32x32x32_bf16 v[48:63], v[188:191], v[152:159], v239\n\t"
      "s_cmp_eq_u32 s45, 0\n\t"
      "v_exp_f32 v112, v112\n\t"
      "v_exp_f32 v113, v113\n\t"
      "v_exp_f32 v114, v114\n\t"
      "v_exp_f32 v115, v115\n\t"
      "v_mfma_f32_32x32x16_bf16 v[128:143], v[160:163], v[80:83], 0\n\t"
      "s_cselect_b64 vcc, -1, 0\n\t"
      "v_exp_f32 v116, v116\n\t"
      "v_exp_f32 v117, v117\n\t"
      "v_exp_f32 v118, v118\n\t"
      "v_exp_f32 v119, v119\n\t"
      "v_mfma_f32_32x32x16_bf16 v[128:143], v[164:167], v[84:87], v[128:143]\n\t"
      "s_waitcnt lgkmcnt(0)\n\t"
      "v_exp_f32 v120, v120\n\t"
      "v_exp_f32 v121, v121\n\t"
      "v_exp_f32 v122, v122\n\t"
      "v_exp_f32 v123, v123\n\t"
      "v_smfmac_f32_32x32x32_bf16 v[0:15], v[176:179], v[96:103], v239\n\t"
      "v_exp_f32 v124, v124\n\t"
      "v_exp_f32 v125, v125\n\t"
      "v_exp_f32 v126, v126\n\t"
      "v_exp_f32 v127, v127\n\t"
      "v_smfmac_f32_32x32x32_bf16 v[0:15], v[180:183], v[104:111], v239\n\t"
      "v_exp_f32 v128, v128\n\t"
      "v_exp_f32 v129, v129\n\t"
      "v_exp_f32 v130, v130\n\t"
      "v_exp_f32 v131, v131\n\t"
      "ds_read_b64_tr_b16 v[184:185], v225 offset:28672\n\t"
      "ds_read_b64_tr_b16 v[186:187], v225 offset:28928\n\t"
      "ds_read_b128 v[168:171], v224 offset:28672\n\t"
      "v_mfma_f32_32x32x16_bf16 v[144:159], v[160:163], v[88:91], 0\n\t"
      "v_exp_f32 v132, v132\n\t"
      "v_exp_f32 v133, v133\n\t"
      "v_exp_f32 v134, v134\n\t"
      "v_exp_f32 v135, v135\n\t"
      "ds_read_b64_tr_b16 v[188:189], v225 offset:29696\n\t"
      "ds_read_b128 v[172:175], v224 offset:29696\n\t"
      "ds_read_b64_tr_b16 v[190:191], v225 offset:29952\n\t"
      "v_mfma_f32_32x32x16_bf16 v[144:159], v[164:167], v[92:95], v[144:159]\n\t"
      "v_exp_f32 v136, v136\n\t"
      "v_exp_f32 v137, v137\n\t"
      "v_exp_f32 v138, v138\n\t"
      "v_exp_f32 v139, v139\n\t"
      "v_smfmac_f32_32x32x32_bf16 v[16:31], v[176:179], v[112:119], v239\n\t"
      "v_exp_f32 v140, v140\n\t"
      "v_exp_f32 v141, v141\n\t"
      "v_exp_f32 v142, v142\n\t"
      "v_exp_f32 v143, v143\n\t"
      "v_smfmac_f32_32x32x32_bf16 v[16:31], v[180:183], v[120:127], v239\n\t"
      "s_waitcnt lgkmcnt(1)\n\t"
      "v_exp_f32 v144, v144\n\t"
      "v_exp_f32 v145, v145\n\t"
      "v_exp_f32 v146, v146\n\t"
      "v_exp_f32 v147, v147\n\t"
      "v_mfma_f32_32x32x16_bf16 v[96:111], v[168:171], v[64:67], 0\n\t"
      "v_exp_f32 v148, v148\n\t"
      "v_exp_f32 v149, v149\n\t"
      "v_exp_f32 v150, v150\n\t"
      "v_exp_f32 v151, v151\n\t"
      "v_mfma_f32_32x32x16_bf16 v[96:111], v[172:175], v[68:71], v[96:111]\n\t"
      "v_exp_f32 v152, v152\n\t"
      "v_exp_f32 v153, v153\n\t"
      "v_exp_f32 v154, v154\n\t"
      "v_exp_f32 v155, v155\n\t"
      "v_smfmac_f32_32x32x32_bf16 v[32:47], v[176:179], v[128:135], v239\n\t"
      "v_exp_f32 v156, v156\n\t"
      "v_exp_f32 v157, v157\n\t"
      "v_exp_f32 v158, v158\n\t"
      "v_exp_f32 v159, v159\n\t"
      "v_add_u32 v224, s46, v224\n\t"
      "v_add_u32 v225, s46, v225\n\t"
      "v_smfmac_f32_32x32x32_bf16 v[32:47], v[180:183], v[136:143], v239\n\t"
      "s_branch .Lqk_OUTER\n\t"
      ".Lqk_TAIL:\n\t"
      "v_exp_f32 v96, v96\n\t"
      "v_exp_f32 v97, v97\n\t"
      "v_exp_f32 v98, v98\n\t"
      "v_exp_f32 v99, v99\n\t"
      "v_mfma_f32_32x32x16_bf16 v[112:127], v[160:163], v[72:75], 0\n\t"
      "ds_read_b128 v[168:171], v224 offset:20480\n\t"
      "v_exp_f32 v100, v100\n\t"
      "v_exp_f32 v101, v101\n\t"
      "v_exp_f32 v102, v102\n\t"
      "v_exp_f32 v103, v103\n\t"
      "v_mfma_f32_32x32x16_bf16 v[112:127], v[164:167], v[76:79], v[112:127]\n\t"
      "ds_read_b128 v[172:175], v224 offset:21504\n\t"
      "v_exp_f32 v104, v104\n\t"
      "v_exp_f32 v105, v105\n\t"
      "v_exp_f32 v106, v106\n\t"
      "v_exp_f32 v107, v107\n\t"
      "v_smfmac_f32_32x32x32_bf16 v[48:63], v[184:187], v[144:151], v239\n\t"
      "ds_read_b128 v[192:195], v224 offset:24576\n\t"
      "v_exp_f32 v108, v108\n\t"
      "v_exp_f32 v109, v109\n\t"
      "v_exp_f32 v110, v110\n\t"
      "v_exp_f32 v111, v111\n\t"
      "v_smfmac_f32_32x32x32_bf16 v[48:63], v[188:191], v[152:159], v239\n\t"
      "ds_read_b128 v[196:199], v224 offset:25600\n\t"
      "s_waitcnt lgkmcnt(3)\n\t"
      "v_exp_f32 v112, v112\n\t"
      "v_exp_f32 v113, v113\n\t"
      "v_exp_f32 v114, v114\n\t"
      "v_exp_f32 v115, v115\n\t"
      "v_mfma_f32_32x32x16_bf16 v[128:143], v[168:171], v[64:67], 0\n\t"
      "ds_read_b128 v[208:211], v224 offset:28672\n\t"
      "s_waitcnt lgkmcnt(3)\n\t"
      "v_exp_f32 v116, v116\n\t"
      "v_exp_f32 v117, v117\n\t"
      "v_exp_f32 v118, v118\n\t"
      "v_exp_f32 v119, v119\n\t"
      "v_mfma_f32_32x32x16_bf16 v[128:143], v[172:175], v[68:71], v[128:143]\n\t"
      "ds_read_b128 v[212:215], v224 offset:29696\n\t"
      "v_exp_f32 v120, v120\n\t"
      "v_exp_f32 v121, v121\n\t"
      "v_exp_f32 v122, v122\n\t"
      "v_exp_f32 v123, v123\n\t"
      "v_smfmac_f32_32x32x32_bf16 v[0:15], v[176:179], v[96:103], v239\n\t"
      "ds_read_b64_tr_b16 v[184:185], v225 offset:20480\n\t"
      "v_exp_f32 v124, v124\n\t"
      "v_exp_f32 v125, v125\n\t"
      "v_exp_f32 v126, v126\n\t"
      "v_exp_f32 v127, v127\n\t"
      "v_smfmac_f32_32x32x32_bf16 v[0:15], v[180:183], v[104:111], v239\n\t"
      "ds_read_b64_tr_b16 v[186:187], v225 offset:20736\n\t"
      "s_waitcnt lgkmcnt(5)\n\t"
      "v_exp_f32 v128, v128\n\t"
      "v_exp_f32 v129, v129\n\t"
      "v_exp_f32 v130, v130\n\t"
      "v_exp_f32 v131, v131\n\t"
      "v_mfma_f32_32x32x16_bf16 v[144:159], v[192:195], v[64:67], 0\n\t"
      "ds_read_b64_tr_b16 v[188:189], v225 offset:21504\n\t"
      "s_waitcnt lgkmcnt(5)\n\t"
      "v_exp_f32 v132, v132\n\t"
      "v_exp_f32 v133, v133\n\t"
      "v_exp_f32 v134, v134\n\t"
      "v_exp_f32 v135, v135\n\t"
      "v_mfma_f32_32x32x16_bf16 v[144:159], v[196:199], v[68:71], v[144:159]\n\t"
      "ds_read_b64_tr_b16 v[190:191], v225 offset:21760\n\t"
      "v_exp_f32 v136, v136\n\t"
      "v_exp_f32 v137, v137\n\t"
      "v_exp_f32 v138, v138\n\t"
      "v_exp_f32 v139, v139\n\t"
      "v_smfmac_f32_32x32x32_bf16 v[16:31], v[176:179], v[112:119], v239\n\t"
      "ds_read_b64_tr_b16 v[200:201], v225 offset:24576\n\t"
      "v_exp_f32 v140, v140\n\t"
      "v_exp_f32 v141, v141\n\t"
      "v_exp_f32 v142, v142\n\t"
      "v_exp_f32 v143, v143\n\t"
      "v_smfmac_f32_32x32x32_bf16 v[16:31], v[180:183], v[120:127], v239\n\t"
      "ds_read_b64_tr_b16 v[202:203], v225 offset:24832\n\t"
      "s_waitcnt lgkmcnt(7)\n\t"
      "v_exp_f32 v144, v144\n\t"
      "v_exp_f32 v145, v145\n\t"
      "v_exp_f32 v146, v146\n\t"
      "v_exp_f32 v147, v147\n\t"
      "v_mfma_f32_32x32x16_bf16 v[96:111], v[208:211], v[64:67], 0\n\t"
      "ds_read_b64_tr_b16 v[204:205], v225 offset:25600\n\t"
      "s_waitcnt lgkmcnt(7)\n\t"
      "v_exp_f32 v148, v148\n\t"
      "v_exp_f32 v149, v149\n\t"
      "v_exp_f32 v150, v150\n\t"
      "v_exp_f32 v151, v151\n\t"
      "v_mfma_f32_32x32x16_bf16 v[96:111], v[212:215], v[68:71], v[96:111]\n\t"
      "ds_read_b64_tr_b16 v[206:207], v225 offset:25856\n\t"
      "s_waitcnt lgkmcnt(6)\n\t"
      "v_exp_f32 v152, v152\n\t"
      "v_exp_f32 v153, v153\n\t"
      "v_exp_f32 v154, v154\n\t"
      "v_exp_f32 v155, v155\n\t"
      "v_smfmac_f32_32x32x32_bf16 v[0:15], v[184:187], v[128:135], v239\n\t"
      "ds_read_b64_tr_b16 v[216:217], v225 offset:28672\n\t"
      "s_waitcnt lgkmcnt(5)\n\t"
      "v_exp_f32 v156, v156\n\t"
      "v_exp_f32 v157, v157\n\t"
      "v_exp_f32 v158, v158\n\t"
      "v_exp_f32 v159, v159\n\t"
      "v_smfmac_f32_32x32x32_bf16 v[0:15], v[188:191], v[136:143], v239\n\t"
      "ds_read_b64_tr_b16 v[218:219], v225 offset:28928\n\t"
      "v_exp_f32 v96, v96\n\t"
      "v_exp_f32 v97, v97\n\t"
      "v_exp_f32 v98, v98\n\t"
      "v_exp_f32 v99, v99\n\t"
      "v_mfma_f32_32x32x16_bf16 v[112:127], v[168:171], v[72:75], 0\n\t"
      "ds_read_b64_tr_b16 v[220:221], v225 offset:29696\n\t"
      "v_exp_f32 v100, v100\n\t"
      "v_exp_f32 v101, v101\n\t"
      "v_exp_f32 v102, v102\n\t"
      "v_exp_f32 v103, v103\n\t"
      "v_mfma_f32_32x32x16_bf16 v[112:127], v[172:175], v[76:79], v[112:127]\n\t"
      "ds_read_b64_tr_b16 v[222:223], v225 offset:29952\n\t"
      "s_waitcnt lgkmcnt(6)\n\t"
      "v_exp_f32 v104, v104\n\t"
      "v_exp_f32 v105, v105\n\t"
      "v_exp_f32 v106, v106\n\t"
      "v_exp_f32 v107, v107\n\t"
      "v_smfmac_f32_32x32x32_bf16 v[0:15], v[200:203], v[144:151], v239\n\t"
      "s_waitcnt lgkmcnt(4)\n\t"
      "v_exp_f32 v108, v108\n\t"
      "v_exp_f32 v109, v109\n\t"
      "v_exp_f32 v110, v110\n\t"
      "v_exp_f32 v111, v111\n\t"
      "v_smfmac_f32_32x32x32_bf16 v[0:15], v[204:207], v[152:159], v239\n\t"
      "v_exp_f32 v112, v112\n\t"
      "v_exp_f32 v113, v113\n\t"
      "v_exp_f32 v114, v114\n\t"
      "v_exp_f32 v115, v115\n\t"
      "v_mfma_f32_32x32x16_bf16 v[128:143], v[192:195], v[72:75], 0\n\t"
      "v_exp_f32 v116, v116\n\t"
      "v_exp_f32 v117, v117\n\t"
      "v_exp_f32 v118, v118\n\t"
      "v_exp_f32 v119, v119\n\t"
      "v_mfma_f32_32x32x16_bf16 v[128:143], v[196:199], v[76:79], v[128:143]\n\t"
      "s_waitcnt lgkmcnt(2)\n\t"
      "v_exp_f32 v120, v120\n\t"
      "v_exp_f32 v121, v121\n\t"
      "v_exp_f32 v122, v122\n\t"
      "v_exp_f32 v123, v123\n\t"
      "v_smfmac_f32_32x32x32_bf16 v[0:15], v[216:219], v[96:103], v239\n\t"
      "s_waitcnt lgkmcnt(0)\n\t"
      "v_exp_f32 v124, v124\n\t"
      "v_exp_f32 v125, v125\n\t"
      "v_exp_f32 v126, v126\n\t"
      "v_exp_f32 v127, v127\n\t"
      "v_smfmac_f32_32x32x32_bf16 v[0:15], v[220:223], v[104:111], v239\n\t"
      "v_exp_f32 v128, v128\n\t"
      "v_exp_f32 v129, v129\n\t"
      "v_exp_f32 v130, v130\n\t"
      "v_exp_f32 v131, v131\n\t"
      "v_mfma_f32_32x32x16_bf16 v[144:159], v[208:211], v[72:75], 0\n\t"
      "v_exp_f32 v132, v132\n\t"
      "v_exp_f32 v133, v133\n\t"
      "v_exp_f32 v134, v134\n\t"
      "v_exp_f32 v135, v135\n\t"
      "v_mov_b32 v234, v12\n\t"
      "v_mfma_f32_32x32x16_bf16 v[144:159], v[212:215], v[76:79], v[144:159]\n\t"
      "v_exp_f32 v136, v136\n\t"
      "v_exp_f32 v137, v137\n\t"
      "v_exp_f32 v138, v138\n\t"
      "v_exp_f32 v139, v139\n\t"
      "v_permlane32_swap_b32 v234, v12\n\t"
      "v_rcp_f32 v234, v234\n\t"
      "v_smfmac_f32_32x32x32_bf16 v[16:31], v[184:187], v[112:119], v239\n\t"
      "v_exp_f32 v140, v140\n\t"
      "v_exp_f32 v141, v141\n\t"
      "v_exp_f32 v142, v142\n\t"
      "v_exp_f32 v143, v143\n\t"
      "v_pk_mul_f32 v[0:1], v[0:1], v[234:235] op_sel_hi:[1,0]\n\t"
      "v_pk_mul_f32 v[2:3], v[2:3], v[234:235] op_sel_hi:[1,0]\n\t"
      "v_pk_mul_f32 v[4:5], v[4:5], v[234:235] op_sel_hi:[1,0]\n\t"
      "v_pk_mul_f32 v[6:7], v[6:7], v[234:235] op_sel_hi:[1,0]\n\t"
      "v_pk_mul_f32 v[8:9], v[8:9], v[234:235] op_sel_hi:[1,0]\n\t"
      "v_pk_mul_f32 v[10:11], v[10:11], v[234:235] op_sel_hi:[1,0]\n\t"
      "v_pk_mul_f32 v[12:13], v[12:13], v[234:235] op_sel_hi:[1,0]\n\t"
      "v_pk_mul_f32 v[14:15], v[14:15], v[234:235] op_sel_hi:[1,0]\n\t"
      "v_cvt_pk_bf16_f32 v0, v0, v1\n\t"
      "v_cvt_pk_bf16_f32 v1, v2, v3\n\t"
      "ds_write_b64 v228, v[0:1] offset:0\n\t"
      "v_cvt_pk_bf16_f32 v2, v4, v5\n\t"
      "v_cvt_pk_bf16_f32 v3, v6, v7\n\t"
      "ds_write_b64 v228, v[2:3] offset:16\n\t"
      "v_cvt_pk_bf16_f32 v4, v8, v9\n\t"
      "v_cvt_pk_bf16_f32 v5, v10, v11\n\t"
      "ds_write_b64 v228, v[4:5] offset:32\n\t"
      "v_cvt_pk_bf16_f32 v6, v12, v13\n\t"
      "v_cvt_pk_bf16_f32 v7, v14, v15\n\t"
      "ds_write_b64 v229, v[6:7] offset:40\n\t"
      "v_smfmac_f32_32x32x32_bf16 v[16:31], v[188:191], v[120:127], v239\n\t"
      "v_exp_f32 v144, v144\n\t"
      "v_exp_f32 v145, v145\n\t"
      "v_exp_f32 v146, v146\n\t"
      "v_exp_f32 v147, v147\n\t"
      "v_mfma_f32_32x32x16_bf16 v[96:111], v[160:163], v[80:83], 0\n\t"
      "v_exp_f32 v148, v148\n\t"
      "v_exp_f32 v149, v149\n\t"
      "v_exp_f32 v150, v150\n\t"
      "v_exp_f32 v151, v151\n\t"
      "v_mfma_f32_32x32x16_bf16 v[96:111], v[164:167], v[84:87], v[96:111]\n\t"
      "s_waitcnt lgkmcnt(0)\n\t"
      "ds_read_b128 v[8:11], v240 offset:0\n\t"
      "v_exp_f32 v152, v152\n\t"
      "v_exp_f32 v153, v153\n\t"
      "v_exp_f32 v154, v154\n\t"
      "v_exp_f32 v155, v155\n\t"
      "v_smfmac_f32_32x32x32_bf16 v[16:31], v[200:203], v[128:135], v239\n\t"
      "v_exp_f32 v156, v156\n\t"
      "v_exp_f32 v157, v157\n\t"
      "v_exp_f32 v158, v158\n\t"
      "v_exp_f32 v159, v159\n\t"
      "v_smfmac_f32_32x32x32_bf16 v[16:31], v[204:207], v[136:143], v239\n\t"
      "v_exp_f32 v96, v96\n\t"
      "v_exp_f32 v97, v97\n\t"
      "v_exp_f32 v98, v98\n\t"
      "v_exp_f32 v99, v99\n\t"
      "v_mfma_f32_32x32x16_bf16 v[112:127], v[168:171], v[80:83], 0\n\t"
      "s_waitcnt lgkmcnt(0)\n\t"
      "global_store_dwordx4 v241, v[8:11], s[50:51] offset:-3072 sc0 sc1\n\t"
      "v_exp_f32 v100, v100\n\t"
      "v_exp_f32 v101, v101\n\t"
      "v_exp_f32 v102, v102\n\t"
      "v_exp_f32 v103, v103\n\t"
      "v_mfma_f32_32x32x16_bf16 v[112:127], v[172:175], v[84:87], v[112:127]\n\t"
      "v_exp_f32 v104, v104\n\t"
      "v_exp_f32 v105, v105\n\t"
      "v_exp_f32 v106, v106\n\t"
      "v_exp_f32 v107, v107\n\t"
      "v_smfmac_f32_32x32x32_bf16 v[16:31], v[216:219], v[144:151], v239\n\t"
      "v_exp_f32 v108, v108\n\t"
      "v_exp_f32 v109, v109\n\t"
      "v_exp_f32 v110, v110\n\t"
      "v_exp_f32 v111, v111\n\t"
      "v_smfmac_f32_32x32x32_bf16 v[16:31], v[220:223], v[152:159], v239\n\t"
      "v_exp_f32 v112, v112\n\t"
      "v_exp_f32 v113, v113\n\t"
      "v_exp_f32 v114, v114\n\t"
      "v_exp_f32 v115, v115\n\t"
      "v_mfma_f32_32x32x16_bf16 v[128:143], v[192:195], v[80:83], 0\n\t"
      "v_exp_f32 v116, v116\n\t"
      "v_exp_f32 v117, v117\n\t"
      "v_exp_f32 v118, v118\n\t"
      "v_exp_f32 v119, v119\n\t"
      "v_mov_b32 v234, v28\n\t"
      "v_mfma_f32_32x32x16_bf16 v[128:143], v[196:199], v[84:87], v[128:143]\n\t"
      "v_exp_f32 v120, v120\n\t"
      "v_exp_f32 v121, v121\n\t"
      "v_exp_f32 v122, v122\n\t"
      "v_exp_f32 v123, v123\n\t"
      "v_permlane32_swap_b32 v234, v28\n\t"
      "v_rcp_f32 v234, v234\n\t"
      "v_smfmac_f32_32x32x32_bf16 v[32:47], v[176:179], v[96:103], v239\n\t"
      "v_exp_f32 v124, v124\n\t"
      "v_exp_f32 v125, v125\n\t"
      "v_exp_f32 v126, v126\n\t"
      "v_exp_f32 v127, v127\n\t"
      "v_pk_mul_f32 v[16:17], v[16:17], v[234:235] op_sel_hi:[1,0]\n\t"
      "v_pk_mul_f32 v[18:19], v[18:19], v[234:235] op_sel_hi:[1,0]\n\t"
      "v_pk_mul_f32 v[20:21], v[20:21], v[234:235] op_sel_hi:[1,0]\n\t"
      "v_pk_mul_f32 v[22:23], v[22:23], v[234:235] op_sel_hi:[1,0]\n\t"
      "v_pk_mul_f32 v[24:25], v[24:25], v[234:235] op_sel_hi:[1,0]\n\t"
      "v_pk_mul_f32 v[26:27], v[26:27], v[234:235] op_sel_hi:[1,0]\n\t"
      "v_pk_mul_f32 v[28:29], v[28:29], v[234:235] op_sel_hi:[1,0]\n\t"
      "v_pk_mul_f32 v[30:31], v[30:31], v[234:235] op_sel_hi:[1,0]\n\t"
      "v_cvt_pk_bf16_f32 v16, v16, v17\n\t"
      "v_cvt_pk_bf16_f32 v17, v18, v19\n\t"
      "ds_write_b64 v228, v[16:17] offset:1792\n\t"
      "v_cvt_pk_bf16_f32 v18, v20, v21\n\t"
      "v_cvt_pk_bf16_f32 v19, v22, v23\n\t"
      "ds_write_b64 v228, v[18:19] offset:1808\n\t"
      "v_cvt_pk_bf16_f32 v20, v24, v25\n\t"
      "v_cvt_pk_bf16_f32 v21, v26, v27\n\t"
      "ds_write_b64 v228, v[20:21] offset:1824\n\t"
      "v_cvt_pk_bf16_f32 v22, v28, v29\n\t"
      "v_cvt_pk_bf16_f32 v23, v30, v31\n\t"
      "ds_write_b64 v229, v[22:23] offset:1832\n\t"
      "v_smfmac_f32_32x32x32_bf16 v[32:47], v[180:183], v[104:111], v239\n\t"
      "v_exp_f32 v128, v128\n\t"
      "v_exp_f32 v129, v129\n\t"
      "v_exp_f32 v130, v130\n\t"
      "v_exp_f32 v131, v131\n\t"
      "v_mfma_f32_32x32x16_bf16 v[144:159], v[208:211], v[80:83], 0\n\t"
      "v_exp_f32 v132, v132\n\t"
      "v_exp_f32 v133, v133\n\t"
      "v_exp_f32 v134, v134\n\t"
      "v_exp_f32 v135, v135\n\t"
      "v_mfma_f32_32x32x16_bf16 v[144:159], v[212:215], v[84:87], v[144:159]\n\t"
      "s_waitcnt lgkmcnt(0)\n\t"
      "ds_read_b128 v[24:27], v240 offset:1024\n\t"
      "v_exp_f32 v136, v136\n\t"
      "v_exp_f32 v137, v137\n\t"
      "v_exp_f32 v138, v138\n\t"
      "v_exp_f32 v139, v139\n\t"
      "v_smfmac_f32_32x32x32_bf16 v[32:47], v[184:187], v[112:119], v239\n\t"
      "ds_read_b128 v[28:31], v240 offset:2048\n\t"
      "v_exp_f32 v140, v140\n\t"
      "v_exp_f32 v141, v141\n\t"
      "v_exp_f32 v142, v142\n\t"
      "v_exp_f32 v143, v143\n\t"
      "v_smfmac_f32_32x32x32_bf16 v[32:47], v[188:191], v[120:127], v239\n\t"
      "v_exp_f32 v144, v144\n\t"
      "v_exp_f32 v145, v145\n\t"
      "v_exp_f32 v146, v146\n\t"
      "v_exp_f32 v147, v147\n\t"
      "v_mfma_f32_32x32x16_bf16 v[96:111], v[160:163], v[88:91], 0\n\t"
      "s_waitcnt lgkmcnt(1)\n\t"
      "global_store_dwordx4 v241, v[24:27], s[50:51] offset:-2048 sc0 sc1\n\t"
      "v_exp_f32 v148, v148\n\t"
      "v_exp_f32 v149, v149\n\t"
      "v_exp_f32 v150, v150\n\t"
      "v_exp_f32 v151, v151\n\t"
      "v_mfma_f32_32x32x16_bf16 v[96:111], v[164:167], v[92:95], v[96:111]\n\t"
      "s_waitcnt lgkmcnt(0)\n\t"
      "global_store_dwordx4 v241, v[28:31], s[50:51] offset:-1024 sc0 sc1\n\t"
      "v_exp_f32 v152, v152\n\t"
      "v_exp_f32 v153, v153\n\t"
      "v_exp_f32 v154, v154\n\t"
      "v_exp_f32 v155, v155\n\t"
      "v_smfmac_f32_32x32x32_bf16 v[32:47], v[200:203], v[128:135], v239\n\t"
      "v_exp_f32 v156, v156\n\t"
      "v_exp_f32 v157, v157\n\t"
      "v_exp_f32 v158, v158\n\t"
      "v_exp_f32 v159, v159\n\t"
      "v_smfmac_f32_32x32x32_bf16 v[32:47], v[204:207], v[136:143], v239\n\t"
      "v_exp_f32 v96, v96\n\t"
      "v_exp_f32 v97, v97\n\t"
      "v_exp_f32 v98, v98\n\t"
      "v_exp_f32 v99, v99\n\t"
      "v_mfma_f32_32x32x16_bf16 v[112:127], v[168:171], v[88:91], 0\n\t"
      "v_exp_f32 v100, v100\n\t"
      "v_exp_f32 v101, v101\n\t"
      "v_exp_f32 v102, v102\n\t"
      "v_exp_f32 v103, v103\n\t"
      "v_mfma_f32_32x32x16_bf16 v[112:127], v[172:175], v[92:95], v[112:127]\n\t"
      "v_exp_f32 v104, v104\n\t"
      "v_exp_f32 v105, v105\n\t"
      "v_exp_f32 v106, v106\n\t"
      "v_exp_f32 v107, v107\n\t"
      "v_smfmac_f32_32x32x32_bf16 v[32:47], v[216:219], v[144:151], v239\n\t"
      "v_exp_f32 v108, v108\n\t"
      "v_exp_f32 v109, v109\n\t"
      "v_exp_f32 v110, v110\n\t"
      "v_exp_f32 v111, v111\n\t"
      "v_smfmac_f32_32x32x32_bf16 v[32:47], v[220:223], v[152:159], v239\n\t"
      "v_exp_f32 v112, v112\n\t"
      "v_exp_f32 v113, v113\n\t"
      "v_exp_f32 v114, v114\n\t"
      "v_exp_f32 v115, v115\n\t"
      "v_mfma_f32_32x32x16_bf16 v[128:143], v[192:195], v[88:91], 0\n\t"
      "v_exp_f32 v116, v116\n\t"
      "v_exp_f32 v117, v117\n\t"
      "v_exp_f32 v118, v118\n\t"
      "v_exp_f32 v119, v119\n\t"
      "v_mov_b32 v234, v44\n\t"
      "v_mfma_f32_32x32x16_bf16 v[128:143], v[196:199], v[92:95], v[128:143]\n\t"
      "v_exp_f32 v120, v120\n\t"
      "v_exp_f32 v121, v121\n\t"
      "v_exp_f32 v122, v122\n\t"
      "v_exp_f32 v123, v123\n\t"
      "v_permlane32_swap_b32 v234, v44\n\t"
      "v_rcp_f32 v234, v234\n\t"
      "v_smfmac_f32_32x32x32_bf16 v[48:63], v[176:179], v[96:103], v239\n\t"
      "v_exp_f32 v124, v124\n\t"
      "v_exp_f32 v125, v125\n\t"
      "v_exp_f32 v126, v126\n\t"
      "v_exp_f32 v127, v127\n\t"
      "v_pk_mul_f32 v[32:33], v[32:33], v[234:235] op_sel_hi:[1,0]\n\t"
      "v_pk_mul_f32 v[34:35], v[34:35], v[234:235] op_sel_hi:[1,0]\n\t"
      "v_pk_mul_f32 v[36:37], v[36:37], v[234:235] op_sel_hi:[1,0]\n\t"
      "v_pk_mul_f32 v[38:39], v[38:39], v[234:235] op_sel_hi:[1,0]\n\t"
      "v_pk_mul_f32 v[40:41], v[40:41], v[234:235] op_sel_hi:[1,0]\n\t"
      "v_pk_mul_f32 v[42:43], v[42:43], v[234:235] op_sel_hi:[1,0]\n\t"
      "v_pk_mul_f32 v[44:45], v[44:45], v[234:235] op_sel_hi:[1,0]\n\t"
      "v_pk_mul_f32 v[46:47], v[46:47], v[234:235] op_sel_hi:[1,0]\n\t"
      "v_cvt_pk_bf16_f32 v32, v32, v33\n\t"
      "v_cvt_pk_bf16_f32 v33, v34, v35\n\t"
      "ds_write_b64 v228, v[32:33] offset:3584\n\t"
      "v_cvt_pk_bf16_f32 v34, v36, v37\n\t"
      "v_cvt_pk_bf16_f32 v35, v38, v39\n\t"
      "ds_write_b64 v228, v[34:35] offset:3600\n\t"
      "v_cvt_pk_bf16_f32 v36, v40, v41\n\t"
      "v_cvt_pk_bf16_f32 v37, v42, v43\n\t"
      "ds_write_b64 v228, v[36:37] offset:3616\n\t"
      "v_cvt_pk_bf16_f32 v38, v44, v45\n\t"
      "v_cvt_pk_bf16_f32 v39, v46, v47\n\t"
      "ds_write_b64 v229, v[38:39] offset:3624\n\t"
      "v_smfmac_f32_32x32x32_bf16 v[48:63], v[180:183], v[104:111], v239\n\t"
      "v_exp_f32 v128, v128\n\t"
      "v_exp_f32 v129, v129\n\t"
      "v_exp_f32 v130, v130\n\t"
      "v_exp_f32 v131, v131\n\t"
      "v_mfma_f32_32x32x16_bf16 v[144:159], v[208:211], v[88:91], 0\n\t"
      "v_exp_f32 v132, v132\n\t"
      "v_exp_f32 v133, v133\n\t"
      "v_exp_f32 v134, v134\n\t"
      "v_exp_f32 v135, v135\n\t"
      "v_mfma_f32_32x32x16_bf16 v[144:159], v[212:215], v[92:95], v[144:159]\n\t"
      "s_waitcnt lgkmcnt(0)\n\t"
      "ds_read_b128 v[40:43], v240 offset:3072\n\t"
      "v_exp_f32 v136, v136\n\t"
      "v_exp_f32 v137, v137\n\t"
      "v_exp_f32 v138, v138\n\t"
      "v_exp_f32 v139, v139\n\t"
      "v_smfmac_f32_32x32x32_bf16 v[48:63], v[184:187], v[112:119], v239\n\t"
      "ds_read_b128 v[44:47], v240 offset:4096\n\t"
      "v_exp_f32 v140, v140\n\t"
      "v_exp_f32 v141, v141\n\t"
      "v_exp_f32 v142, v142\n\t"
      "v_exp_f32 v143, v143\n\t"
      "v_smfmac_f32_32x32x32_bf16 v[48:63], v[188:191], v[120:127], v239\n\t"
      "v_exp_f32 v144, v144\n\t"
      "v_exp_f32 v145, v145\n\t"
      "v_exp_f32 v146, v146\n\t"
      "v_exp_f32 v147, v147\n\t"
      "v_smfmac_f32_32x32x32_bf16 v[48:63], v[200:203], v[128:135], v239\n\t"
      "s_waitcnt lgkmcnt(1)\n\t"
      "global_store_dwordx4 v241, v[40:43], s[50:51] offset:0 sc0 sc1\n\t"
      "v_exp_f32 v148, v148\n\t"
      "v_exp_f32 v149, v149\n\t"
      "v_exp_f32 v150, v150\n\t"
      "v_exp_f32 v151, v151\n\t"
      "v_smfmac_f32_32x32x32_bf16 v[48:63], v[204:207], v[136:143], v239\n\t"
      "s_waitcnt lgkmcnt(0)\n\t"
      "global_store_dwordx4 v241, v[44:47], s[50:51] offset:1024 sc0 sc1\n\t"
      "v_exp_f32 v152, v152\n\t"
      "v_exp_f32 v153, v153\n\t"
      "v_exp_f32 v154, v154\n\t"
      "v_exp_f32 v155, v155\n\t"
      "v_smfmac_f32_32x32x32_bf16 v[48:63], v[216:219], v[144:151], v239\n\t"
      "v_exp_f32 v156, v156\n\t"
      "v_exp_f32 v157, v157\n\t"
      "v_exp_f32 v158, v158\n\t"
      "v_exp_f32 v159, v159\n\t"
      "s_nop 1\n\t"
      "v_smfmac_f32_32x32x32_bf16 v[48:63], v[220:223], v[152:159], v239\n\t"
      "s_nop 11\n\t"
      "v_mov_b32 v234, v60\n\t"
      "s_nop 1\n\t"
      "v_permlane32_swap_b32 v234, v60\n\t"
      "v_rcp_f32 v234, v234\n\t"
      "s_nop 0\n\t"
      "v_pk_mul_f32 v[48:49], v[48:49], v[234:235] op_sel_hi:[1,0]\n\t"
      "v_pk_mul_f32 v[50:51], v[50:51], v[234:235] op_sel_hi:[1,0]\n\t"
      "v_pk_mul_f32 v[52:53], v[52:53], v[234:235] op_sel_hi:[1,0]\n\t"
      "v_pk_mul_f32 v[54:55], v[54:55], v[234:235] op_sel_hi:[1,0]\n\t"
      "v_pk_mul_f32 v[56:57], v[56:57], v[234:235] op_sel_hi:[1,0]\n\t"
      "v_pk_mul_f32 v[58:59], v[58:59], v[234:235] op_sel_hi:[1,0]\n\t"
      "v_pk_mul_f32 v[60:61], v[60:61], v[234:235] op_sel_hi:[1,0]\n\t"
      "v_pk_mul_f32 v[62:63], v[62:63], v[234:235] op_sel_hi:[1,0]\n\t"
      "v_cvt_pk_bf16_f32 v48, v48, v49\n\t"
      "v_cvt_pk_bf16_f32 v49, v50, v51\n\t"
      "ds_write_b64 v228, v[48:49] offset:5376\n\t"
      "v_cvt_pk_bf16_f32 v50, v52, v53\n\t"
      "v_cvt_pk_bf16_f32 v51, v54, v55\n\t"
      "ds_write_b64 v228, v[50:51] offset:5392\n\t"
      "v_cvt_pk_bf16_f32 v52, v56, v57\n\t"
      "v_cvt_pk_bf16_f32 v53, v58, v59\n\t"
      "ds_write_b64 v228, v[52:53] offset:5408\n\t"
      "v_cvt_pk_bf16_f32 v54, v60, v61\n\t"
      "v_cvt_pk_bf16_f32 v55, v62, v63\n\t"
      "ds_write_b64 v229, v[54:55] offset:5416\n\t"
      "s_waitcnt lgkmcnt(0)\n\t"
      "ds_read_b128 v[56:59], v240 offset:5120\n\t"
      "ds_read_b128 v[60:63], v240 offset:6144\n\t"
      "s_waitcnt lgkmcnt(1)\n\t"
      "global_store_dwordx4 v241, v[56:59], s[50:51] offset:2048 sc0 sc1\n\t"
      "s_waitcnt lgkmcnt(0)\n\t"
      "global_store_dwordx4 v241, v[60:63], s[50:51] offset:3072 sc0 sc1\n\t"
      "s_endpgm\n\t"
      ".Lqk_SLOW:\n\t"
      "s_waitcnt vmcnt(0) lgkmcnt(0)\n\t"
      "s_setprio 0\n\t"
      ".Lqk_CODE1:\n\t"
      : "+s"(ka) : "v"(threadIdx.x), "s"(bid)
      : "memory", "scc", "vcc", "v0", "v1", "v2", "v3", "v4", "v5", "v6", "v7", "v8", "v9", "v10", "v11", "v12", "v13", "v14", "v15", "v16", "v17", "v18", "v19", "v20", "v21", "v22", "v23", "v24", "v25", "v26", "v27", "v28", "v29", "v30", "v31", "v32", "v33", "v34", "v35", "v36", "v37", "v38", "v39", "v40", "v41", "v42", "v43", "v44", "v45", "v46", "v47", "v48", "v49", "v50", "v51", "v52", "v53", "v54", "v55", "v56", "v57", "v58", "v59", "v60", "v61", "v62", "v63", "v64", "v65", "v66", "v67", "v68", "v69", "v70", "v71", "v72", "v73", "v74", "v75", "v76", "v77", "v78", "v79", "v80", "v81", "v82", "v83", "v84", "v85", "v86", "v87", "v88", "v89", "v90", "v91", "v92", "v93", "v94", "v95", "v96", "v97", "v98", "v99", "v100", "v101", "v102", "v103", "v104", "v105", "v106", "v107", "v108", "v109", "v110", "v111", "v112", "v113", "v114", "v115", "v116", "v117", "v118", "v119", "v120", "v121", "v122", "v123", "v124", "v125", "v126", "v127", "v128", "v129", "v130", "v131", "v132", "v133", "v134", "v135", "v136", "v137", "v138", "v139", "v140", "v141", "v142", "v143", "v144", "v145", "v146", "v147", "v148", "v149", "v150", "v151", "v152", "v153", "v154", "v155", "v156", "v157", "v158", "v159", "v160", "v161", "v162", "v163", "v164", "v165", "v166", "v167", "v168", "v169", "v170", "v171", "v172", "v173", "v174", "v175", "v176", "v177", "v178", "v179", "v180", "v181", "v182", "v183", "v184", "v185", "v186", "v187", "v188", "v189", "v190", "v191", "v192", "v193", "v194", "v195", "v196", "v197", "v198", "v199", "v200", "v201", "v202", "v203", "v204", "v205", "v206", "v207", "v208", "v209", "v210", "v211", "v212", "v213", "v214", "v215", "v216", "v217", "v218", "v219", "v220", "v221", "v222", "v223", "v224", "v225", "v226", "v227", "v228", "v229", "v230", "v231", "v232", "v233", "v234", "v235", "v236", "v237", "v238", "v239", "v240", "v241", "v242", "v243", "v244", "v245", "v246", "v247", "v248", "v249", "v250", "s40", "s41", "s42", "s43", "s44", "s45", "s46", "s47", "s48", "s49", "s50", "s51", "s52", "s53", "s54", "s55", "s56", "s57", "s58", "s59", "s60", "s61", "s62", "s63", "s64", "s65", "s66", "s67", "s68", "s69", "s70", "s71", "s72", "s73", "s74", "s75", "s76", "s77", "s78", "s79", "s80", "s81", "s82", "s83", "s84", "s85", "s86", "s87", "s88", "s89", "s90", "s91", "s92", "s93", "s94", "s95");
  const int head = (bid & 7) * 8 + (bid >> 5), q0 = ((bid >> 3) & 3) * 512;
  const unsigned hoff = (unsigned)head * (SEQ * ROWB), qoff = hoff + q0 * ROWB;
  const unsigned tw = ((const unsigned*)ka[3])[(head & (NHEAD - 1)) >> 1];
  const float c = __uint_as_float((head & 1) ? (tw & 0xffff0000u) : (tw << 16)) * LOG2E;
  slow_path((const char*)ka[0] + qoff, (const char*)ka[1] + hoff, (const char*)ka[2] + hoff, (char*)ka[4] + qoff, c, (LDSP(char))smem);
}

extern "C" void kernel_launch(void* const* d_in, const int* in_sizes, int n_in, void* d_out, int out_size, void* d_ws, size_t ws_size,
                              hipStream_t stream) {
  (void)hipFuncSetAttribute((const void*)qkn_attn, hipFuncAttributeMaxDynamicSharedMemorySize, LDS_BYTES);
  qkn_attn<<<256, 512, LDS_BYTES, stream>>>((const char*)d_in[0], (const char*)d_in[1], (const char*)d_in[2],
                                            (const unsigned*)d_in[3], (char*)d_out);
}
```
